```python
import torch

def _softmax(x, axis=-1):
    return torch.softmax(x, dim=axis)
def _gelu(x):
    return torch.nn.functional.gelu(x, approximate="tanh")
def _silu(x):
    return torch.nn.functional.silu(x)
def _sigmoid(x):
    return torch.sigmoid(x)
def _layernorm(x, axis=-1, eps=1e-5):
    m = x.mean(dim=axis, keepdim=True)
    v = x.var(dim=axis, keepdim=True, unbiased=False)
    return (x - m) / torch.sqrt(v + eps)
def _scatter_add(tgt, idx, src, axis=0):
    return tgt.clone().index_add_(axis, idx, src)
def _segment_sum(src, seg, n):
    out = torch.zeros((n,) + src.shape[1:], dtype=src.dtype)
    return out.index_add_(0, seg, src)
def _pad(x, pw):
    flat = tuple(v for pair in reversed(pw) for v in pair)
    return torch.nn.functional.pad(x, flat)

kernel_name = 'standalone__structured_sparse_n_m__ragged'

def setup_inputs():

    _w = (lambda a: (lambda t: t.to(torch.bfloat16) if t.is_floating_point() else t)(torch.as_tensor(a)))
    B, S, D = 4, 4096, 1024
    assert D % 4 == 0, "d_model must be divisible by M=4 for 2:4 sparsity"
    return {
        "x": _w((lambda *s: torch.randn(*s, dtype=torch.bfloat16) * 0.05)(B, S, D)),
        "w": _w((lambda *s: torch.randn(*s, dtype=torch.bfloat16) * 0.05)(D, D)),
        "b": _w((lambda *s: torch.randn(*s, dtype=torch.bfloat16) * 0.05)(D)),
    }

def reference(x, w, b):
    B, S, D = x.shape
    N, M = 2, 4
    G = D // M
    wg = w.reshape(G, M, D)
    mag = torch.abs(wg)
    order = torch.argsort(mag, 1)
    thr = (lambda x, i, axis: torch.take_along_dim(x, i, dim=axis))(mag, order[:, M - N:M - N + 1, :], 1)
    mask = (lambda x, dt: x.to(dt))(mag >= thr, w.dtype)
    wp = (wg * mask).reshape(D, D)
    return torch.einsum("bsd,df->bsf", x, wp) + b
```

```cpp
#include <hip/hip_runtime.h>
#include <hip/hip_bf16.h>
#include <stdint.h>

typedef __attribute__((ext_vector_type(8)))  __bf16 bf16x8;
typedef __attribute__((ext_vector_type(16))) __bf16 bf16x16;
typedef __attribute__((ext_vector_type(4)))  float  f32x4;
typedef __attribute__((ext_vector_type(4)))  unsigned u32x4;
typedef __attribute__((ext_vector_type(2)))  unsigned u32x2;
typedef __attribute__((ext_vector_type(8)))  unsigned u32x8;

constexpr int MROWS = 16384, KD = 1024, ND = 1024;
constexpr int NSLAB = 16;
constexpr int MAXROUND = 16;
constexpr int FASTROUND = 3;

constexpr size_t WS_WA = 0;
constexpr size_t WS_WI = WS_WA + (size_t)NSLAB * 16 * 4 * 1024;
constexpr size_t WS_RA = WS_WI + (size_t)NSLAB * 8 * 1024;
constexpr size_t WS_RI = WS_RA + (size_t)NSLAB * 4 * MAXROUND * 1024;
constexpr size_t WS_RG = WS_RI + (size_t)NSLAB * 4 * MAXROUND * 256;
constexpr size_t WS_RN = WS_RG + (size_t)NSLAB * 4 * MAXROUND * 16;
constexpr size_t WS_END = WS_RN + NSLAB * 4 * 4;

__global__ __launch_bounds__(1024) void prep_kernel(const uint16_t* __restrict__ w, unsigned char* __restrict__ ws) {
  __shared__ __attribute__((aligned(16))) unsigned char smem[32 * 256 * 4 * 2 + 32 * 256 + 2 * 256 + 2 * 256 + 64];
  unsigned* LM = (unsigned*)smem;
  unsigned* LR = LM + 32 * 256;
  unsigned char* LI = (unsigned char*)(LR + 32 * 256);
  unsigned char* FL = LI + 32 * 256;
  unsigned char* LS = FL + 2 * 256;
  unsigned* CN = (unsigned*)(LS + 2 * 256);
  const int tid = threadIdx.x, blk = blockIdx.x, slab = blk >> 1, u = blk & 1;
  const int G = tid >> 2, o = tid & 3;
  const int c0 = blk * 32 + o * 8;
  u32x4 r[4];
#pragma unroll
  for (int j = 0; j < 4; ++j) r[j] = *(const u32x4*)(w + (size_t)(4 * G + j) * ND + c0);
  if (tid < 512) FL[tid] = 0;
  __syncthreads();
  bool any0 = false, any1 = false;
#pragma unroll
  for (int e = 0; e < 8; ++e) {
    unsigned v[4], m[4];
#pragma unroll
    for (int j = 0; j < 4; ++j) { v[j] = (r[j][e >> 1] >> ((e & 1) * 16)) & 0xffffu; m[j] = v[j] & 0x7fffu; }
    unsigned val[4] = {0, 0, 0, 0}, pos[4] = {0, 0, 0, 0}, mg[4] = {0, 0, 0, 0};
#pragma unroll
    for (int j = 0; j < 4; ++j) {
      int rank = 0;
#pragma unroll
      for (int l = 0; l < 4; ++l) if (l != j) rank += (m[l] > m[j] || (m[l] == m[j] && l < j)) ? 1 : 0;
#pragma unroll
      for (int q = 0; q < 4; ++q) if (rank == q) { val[q] = v[j]; pos[q] = j; mg[q] = m[j]; }
    }
    const unsigned thr = mg[1];
    const bool k2 = (mg[2] == thr) && (thr != 0), k3 = (mg[3] == thr) && (thr != 0);
    const unsigned mainp = val[0] | (val[1] << 16);
    const unsigned resp = (k2 ? val[2] : 0u) | ((k3 ? val[3] : 0u) << 16);
    const unsigned nib = pos[0] | (pos[1] << 2) | ((k2 ? pos[2] : 0u) << 4) | ((k3 ? pos[3] : 0u) << 6);
    const int col = o * 8 + e;
    LM[col * 256 + G] = mainp; LR[col * 256 + G] = resp; LI[col * 256 + G] = (unsigned char)nib;
    if (e < 4) any0 |= k2; else any1 |= k2;
  }
  if (any0) FL[G] = 1;
  if (any1) FL[256 + G] = 1;
  __syncthreads();
  const int lane = tid & 63, wv = tid >> 6;
  unsigned long long be = 0, bo = 0; bool fl = false; int g2 = tid & 255, tt2 = (tid >> 8) & 1;
  if (tid < 512) {
    fl = FL[tid] != 0;
    be = __ballot(fl && !(g2 & 1)); bo = __ballot(fl && (g2 & 1));
    if (lane == 0) { CN[wv * 2] = __popcll(be); CN[wv * 2 + 1] = __popcll(bo); }
  }
  __syncthreads();
  if (tid < 512) {
    int ne = 0, no = 0, pe = 0, po = 0;
#pragma unroll
    for (int q = 0; q < 4; ++q) {
      int a = CN[(tt2 * 4 + q) * 2], b = CN[(tt2 * 4 + q) * 2 + 1];
      if (q < (wv & 3)) { pe += a; po += b; }
      ne += a; no += b;
    }
    const unsigned long long below = (1ull << lane) - 1;
    const int mn = min(ne, no);
    if (fl) {
      int slot;
      if (g2 & 1) { int i = po + __popcll(bo & below); slot = i < mn ? 2 * i + 1 : mn + i; }
      else        { int i = pe + __popcll(be & below); slot = i < mn ? 2 * i     : mn + i; }
      LS[tt2 * 256 + slot] = (unsigned char)g2;
    }
    if (g2 == 0) ((unsigned*)(ws + WS_RN))[slab * 4 + u * 2 + tt2] = ne + no;
  }
  __syncthreads();
  int nsl[2];
#pragma unroll
  for (int tt = 0; tt < 2; ++tt) { int n = 0;
#pragma unroll
    for (int q = 0; q < 8; ++q) n += CN[tt * 8 + q];
    nsl[tt] = n; }
  {
    const int rr = lane & 15, ga = lane >> 4;
    const int s = wv;
    unsigned idx = 0;
#pragma unroll
    for (int tt = 0; tt < 2; ++tt) {
      const int col = (rr >> 2) * 8 + tt * 4 + (rr & 3);
      const int g0 = 16 * s + 4 * ga;
      const u32x4 a = *(const u32x4*)(LM + col * 256 + g0);
      *(u32x4*)(ws + WS_WA + ((((size_t)slab * 16 + s) * 4 + u * 2 + tt) * 64 + lane) * 16) = a;
      const unsigned nb = *(const unsigned*)(LI + col * 256 + g0);
      const unsigned i16 = (nb & 0xf) | ((nb >> 4) & 0xf0) | ((nb >> 8) & 0xf00) | ((nb >> 12) & 0xf000);
      idx |= i16 << (16 * tt);
    }
    ((unsigned*)(ws + WS_WI))[(((size_t)slab * 8 + (s >> 1)) * 64 + lane) * 4 + (s & 1) * 2 + u] = idx;
#pragma unroll
    for (int tt = 0; tt < 2; ++tt) {
      const int round = wv, n = nsl[tt];
      if (round < FASTROUND || round * 16 < n) {
        const int col = (rr >> 2) * 8 + tt * 4 + (rr & 3);
        u32x4 a; unsigned i16 = 0;
#pragma unroll
        for (int p = 0; p < 4; ++p) {
          const int j = 4 * ga + p;
          const int slot = 16 * round + 4 * ((j & 1) + 2 * (j >> 3)) + ((j & 7) >> 1);
          unsigned av = 0, nb = 0;
          if (slot < n) { const int gg = LS[tt * 256 + slot]; av = LR[col * 256 + gg]; nb = LI[col * 256 + gg] >> 4; }
          a[p] = av; i16 |= nb << (4 * p);
        }
        const size_t rb = ((size_t)slab * 4 + u * 2 + tt) * MAXROUND + round;
        *(u32x4*)(ws + WS_RA + (rb * 64 + lane) * 16) = a;
        ((unsigned*)(ws + WS_RI))[rb * 64 + lane] = i16;
        if (lane < 4) {
          unsigned gp = 0;
#pragma unroll
          for (int q = 0; q < 4; ++q) { const int slot = 16 * round + 4 * q + lane; if (slot < n) gp |= (unsigned)LS[tt * 256 + slot] << (8 * q); }
          ((unsigned*)(ws + WS_RG))[rb * 4 + lane] = gp;
        }
      }
    }
  }
}

constexpr int NSLOT = 5, TILE_B = 16 * 2048, NTILE = 16;

__device__ __forceinline__ bf16x16 mk16(u32x4 lo, u32x4 hi) {
  u32x8 v = {lo[0], lo[1], lo[2], lo[3], hi[0], hi[1], hi[2], hi[3]};
  return __builtin_bit_cast(bf16x16, v);
}
__device__ __forceinline__ unsigned pk2(float a, float b) {
  typedef __attribute__((ext_vector_type(2))) __bf16 bf16x2;
  bf16x2 v = {(__bf16)a, (__bf16)b};
  return __builtin_bit_cast(unsigned, v);
}
__device__ __forceinline__ void dma4(unsigned m0v, unsigned va, unsigned vb, const void* sbase) {
  asm volatile("s_mov_b32 m0, %0\n\ts_nop 0\n\t"
               "global_load_lds_dwordx4 %1, %3\n\t"
               "global_load_lds_dwordx4 %1, %3 offset:1024\n\t"
               "global_load_lds_dwordx4 %2, %3 offset:2048\n\t"
               "global_load_lds_dwordx4 %2, %3 offset:3072"
               :: "s"(m0v), "v"(va), "v"(vb), "s"(sbase) : "memory");
}

__global__ __launch_bounds__(256, 1) void gemm_kernel(const uint16_t* __restrict__ x, const uint16_t* __restrict__ bias,
                                                      const unsigned char* __restrict__ ws, uint16_t* __restrict__ out) {
  extern __shared__ __attribute__((aligned(16))) unsigned char lds[];
  const int tid = threadIdx.x, lane = tid & 63;
  const int wv = __builtin_amdgcn_readfirstlane(tid >> 6);
  const int c = lane & 15, g = lane >> 4;
  const int bid = blockIdx.x;
  const int xcd = bid & 7, jj = bid >> 3;
  const int ns = jj & 3, mg = xcd * 8 + (jj >> 2);
  const int slab = ns * 4 + wv;
  const int row0 = mg * 256;
  const unsigned lds0 = (unsigned)(uintptr_t)lds;

  unsigned voff[4];
#pragma unroll
  for (int r = 0; r < 4; ++r) {
    const int row = 4 * wv + r;
    voff[r] = (unsigned)((4 * wv + (r & 2)) * 2048 + ((lane ^ row) * 16));
  }
  const unsigned char* xb = (const unsigned char*)x + (size_t)row0 * 2048;
  auto issue_tile = [&](int tile, int slot) {
    const unsigned char* sb = xb + (size_t)tile * TILE_B;
    const unsigned m = lds0 + slot * TILE_B + wv * 8192;
    dma4(m, voff[0], voff[1], sb);
    dma4(m + 4096, voff[2], voff[3], sb);
  };
  issue_tile(0, 0);
  issue_tile(1, 1);

  bf16x8 a[16][4]; u32x4 wi[8];
  {
    const u32x4* pa = (const u32x4*)(ws + WS_WA) + (size_t)slab * 16 * 4 * 64 + lane;
    const u32x4* pi = (const u32x4*)(ws + WS_WI) + (size_t)slab * 8 * 64 + lane;
#pragma unroll
    for (int s = 0; s < 16; ++s) {
      if ((s & 1) == 0) wi[s >> 1] = pi[(s >> 1) * 64];
#pragma unroll
      for (int t = 0; t < 4; ++t) a[s][t] = __builtin_bit_cast(bf16x8, pa[(s * 4 + t) * 64]);
    }
  }
  bf16x8 ra[4][FASTROUND]; unsigned ri[4][FASTROUND]; unsigned roff[4][FASTROUND][4]; int rn[4];
#pragma unroll
  for (int t = 0; t < 4; ++t) {
    rn[t] = __builtin_amdgcn_readfirstlane(((const unsigned*)(ws + WS_RN))[slab * 4 + t]);
#pragma unroll
    for (int r = 0; r < FASTROUND; ++r) {
      const size_t rb = ((size_t)slab * 4 + t) * MAXROUND + r;
      ra[t][r] = __builtin_bit_cast(bf16x8, *(const u32x4*)(ws + WS_RA + (rb * 64 + lane) * 16));
      ri[t][r] = ((const unsigned*)(ws + WS_RI))[rb * 64 + lane];
      const unsigned gp = ((const unsigned*)(ws + WS_RG))[rb * 4 + g];
#pragma unroll
      for (int q = 0; q < 4; ++q) {
        const unsigned G = (gp >> (8 * q)) & 0xff;
        roff[t][r][q] = c * 2048 + (((G >> 1) ^ c) * 16) + (G & 1) * 8;
      }
    }
  }
  const int rmax = max(max(rn[0], rn[1]), max(rn[2], rn[3]));
  float bs[4][4];
  {
    const int cb = slab * 64 + 8 * g;
#pragma unroll
    for (int h = 0; h < 2; ++h) {
      const u32x4 bv = *(const u32x4*)(bias + cb + 32 * h);
#pragma unroll
      for (int e = 0; e < 8; ++e) bs[2 * h + (e >> 2)][e & 3] = __uint_as_float(((bv[e >> 1] >> ((e & 1) * 16)) & 0xffffu) << 16);
    }
  }
  issue_tile(2, 2);
  issue_tile(3, 3);

  unsigned boff[4];
#pragma unroll
  for (int v = 0; v < 4; ++v) boff[v] = c * 2048 + (((v * 4 + g) ^ c) * 16);
  auto ldb = [&](unsigned slotbase, int s) -> bf16x16 {
    const unsigned char* p = lds + slotbase + (s >> 1) * 256;
    const u32x4 lo = *(const u32x4*)(p + boff[(s & 1) * 2]);
    const u32x4 hi = *(const u32x4*)(p + boff[(s & 1) * 2 + 1]);
    return mk16(lo, hi);
  };
  unsigned char* op = (unsigned char*)out + ((size_t)(row0 + c) * ND + slab * 64 + 8 * g) * 2;

  __builtin_amdgcn_s_waitcnt(0x0070 | 0x0F00);
  asm volatile("s_waitcnt vmcnt(0)\n\ts_barrier" ::: "memory");

  bf16x16 b0 = ldb(0, 0), b1 = ldb(0, 1), b2;
  int slot = 0;
#pragma unroll 1
  for (int it = 0; it < NTILE; ++it) {
    const unsigned sb = slot * TILE_B;
    const int nslot = slot + 1 == NSLOT ? 0 : slot + 1;
    const unsigned nsb = nslot * TILE_B;
    f32x4 acc[4];
#pragma unroll
    for (int t = 0; t < 4; ++t) acc[t] = f32x4{bs[t][0], bs[t][1], bs[t][2], bs[t][3]};
#pragma unroll
    for (int s = 0; s < 16; ++s) {
      bf16x16 bn = (s + 2 < 16) ? ldb(sb, s + 2) : ldb(nsb, s + 2 - 16);
      const int i0 = (int)wi[s >> 1][(s & 1) * 2], i1 = (int)wi[s >> 1][(s & 1) * 2 + 1];
      acc[0] = __builtin_amdgcn_smfmac_f32_16x16x64_bf16(a[s][0], b0, acc[0], i0, 0, 0);
      acc[1] = __builtin_amdgcn_smfmac_f32_16x16x64_bf16(a[s][1], b0, acc[1], i0, 0, 1);
      acc[2] = __builtin_amdgcn_smfmac_f32_16x16x64_bf16(a[s][2], b0, acc[2], i1, 0, 0);
      acc[3] = __builtin_amdgcn_smfmac_f32_16x16x64_bf16(a[s][3], b0, acc[3], i1, 0, 1);
      b0 = b1; b1 = bn;
      if (s == 7) {
        if (it <= 12)      asm volatile("s_waitcnt vmcnt(22)\n\ts_barrier" ::: "memory");
        else if (it == 13) asm volatile("s_waitcnt vmcnt(14)\n\ts_barrier" ::: "memory");
        else if (it == 14) asm volatile("s_waitcnt vmcnt(6)\n\ts_barrier" ::: "memory");
        if (it + 4 < NTILE) issue_tile(it + 4, slot == 0 ? NSLOT - 1 : slot - 1);
      }
    }
    const unsigned char* tp = lds + sb;
#pragma unroll
    for (int t = 0; t < 4; ++t) {
#pragma unroll
      for (int r = 0; r < FASTROUND; ++r) {
        if (rn[t] > 16 * r) {
          u32x2 q0 = *(const u32x2*)(tp + roff[t][r][0]), q1 = {0, 0}, q2 = {0, 0}, q3 = {0, 0};
          if (rn[t] > 16 * r + 4)  q1 = *(const u32x2*)(tp + roff[t][r][1]);
          if (rn[t] > 16 * r + 8)  q2 = *(const u32x2*)(tp + roff[t][r][2]);
          if (rn[t] > 16 * r + 12) q3 = *(const u32x2*)(tp + roff[t][r][3]);
          const bf16x16 br = mk16(u32x4{q0[0], q0[1], q1[0], q1[1]}, u32x4{q2[0], q2[1], q3[0], q3[1]});
          acc[t] = __builtin_amdgcn_smfmac_f32_16x16x64_bf16(ra[t][r], br, acc[t], (int)ri[t][r], 0, 0);
        }
      }
    }
    if (rmax > 16 * FASTROUND) {
#pragma unroll
      for (int t = 0; t < 4; ++t) {
        for (int r = FASTROUND; 16 * r < rn[t]; ++r) {
          const size_t rb = ((size_t)slab * 4 + t) * MAXROUND + r;
          const bf16x8 xa = __builtin_bit_cast(bf16x8, *(const u32x4*)(ws + WS_RA + (rb * 64 + lane) * 16));
          const unsigned xi = ((const unsigned*)(ws + WS_RI))[rb * 64 + lane];
          const unsigned gp = ((const unsigned*)(ws + WS_RG))[rb * 4 + g];
          u32x2 q[4];
#pragma unroll
          for (int k = 0; k < 4; ++k) {
            const unsigned G = (gp >> (8 * k)) & 0xff;
            q[k] = *(const u32x2*)(tp + c * 2048 + (((G >> 1) ^ c) * 16) + (G & 1) * 8);
          }
          const bf16x16 br = mk16(u32x4{q[0][0], q[0][1], q[1][0], q[1][1]}, u32x4{q[2][0], q[2][1], q[3][0], q[3][1]});
          acc[t] = __builtin_amdgcn_smfmac_f32_16x16x64_bf16(xa, br, acc[t], (int)xi, 0, 0);
        }
      }
    }
    unsigned char* o = op + (size_t)it * 16 * ND * 2;
    asm volatile("" ::: "memory");
    *(u32x4*)(o)      = u32x4{pk2(acc[0][0], acc[0][1]), pk2(acc[0][2], acc[0][3]), pk2(acc[1][0], acc[1][1]), pk2(acc[1][2], acc[1][3])};
    *(u32x4*)(o + 64) = u32x4{pk2(acc[2][0], acc[2][1]), pk2(acc[2][2], acc[2][3]), pk2(acc[3][0], acc[3][1]), pk2(acc[3][2], acc[3][3])};
    asm volatile("" ::: "memory");
    slot = nslot;
  }
}

extern "C" void kernel_launch(void* const* d_in, const int* in_sizes, int n_in,
                              void* d_out, int out_size, void* d_ws, size_t ws_size,
                              hipStream_t stream) {
  const uint16_t* x = (const uint16_t*)d_in[0];
  const uint16_t* w = (const uint16_t*)d_in[1];
  const uint16_t* b = (const uint16_t*)d_in[2];
  (void)hipFuncSetAttribute((const void*)gemm_kernel, hipFuncAttributeMaxDynamicSharedMemorySize, NSLOT * TILE_B);
  prep_kernel<<<32, 1024, 0, stream>>>(w, (unsigned char*)d_ws);
  gemm_kernel<<<256, 256, NSLOT * TILE_B, stream>>>(x, b, (const unsigned char*)d_ws, (uint16_t*)d_out);
}
```

```cpp
#include <hip/hip_runtime.h>
#include <hip/hip_bf16.h>
#include <stdint.h>

typedef __attribute__((ext_vector_type(4)))  unsigned u32x4;
typedef __attribute__((ext_vector_type(2)))  unsigned u32x2;

constexpr int ND = 1024;
constexpr int NSLAB = 16;
constexpr int NREC = 73;
constexpr int REC_FLG = 72;
constexpr int K_ZERO = 512, K_BIAS = 513, RV_ENT = 514;
constexpr int LDS_TOTAL = 139280, ROWB = 8192, LDS_SCR = 131072;

constexpr size_t WS_EPOCH = 0;
constexpr size_t WS_FLAG = 4096;
constexpr size_t WS_BLOB = 16384;
constexpr size_t WS_RV = WS_BLOB + (size_t)NSLAB * NREC * 1024;
constexpr size_t WS_GL = WS_RV + (size_t)NSLAB * 4 * RV_ENT * 64;

__device__ __forceinline__ unsigned mkkey(unsigned v, unsigned j) { return ((v & 0x7fffu) << 3) | (j << 1) | (v >> 15); }
__device__ __forceinline__ unsigned keyval(unsigned k) { return (k >> 3) | ((k & 1u) << 15); }
__device__ __forceinline__ unsigned keypos(unsigned k) { return (k >> 1) & 3u; }
__device__ __forceinline__ void st16(void* p, u32x4 v) { asm volatile("global_store_dwordx4 %0, %1, off sc1\n\ts_nop 1" :: "v"(p), "v"(v) : "memory"); }
__device__ __forceinline__ void st8(void* p, u32x2 v)  { asm volatile("global_store_dwordx2 %0, %1, off sc1" :: "v"(p), "v"(v) : "memory"); }
__device__ __forceinline__ void st4(void* p, unsigned v) { asm volatile("global_store_dword %0, %1, off sc1" :: "v"(p), "v"(v) : "memory"); }

__global__ __launch_bounds__(256, 1) void fused_kernel(const uint16_t* __restrict__ x, const uint16_t* __restrict__ w,
                                                       const uint16_t* __restrict__ bias, unsigned char* __restrict__ ws,
                                                       uint16_t* __restrict__ out) {
  extern __shared__ __attribute__((aligned(16))) unsigned char lds[];

  const int tid = threadIdx.x, lane = tid & 63;
  const int wv = __builtin_amdgcn_readfirstlane(tid >> 6);
  const int bid = blockIdx.x;
  const int xcd = bid & 7, jj = bid >> 3;
  const int ns = jj & 3, mg = xcd * 8 + (jj >> 2);
  const int slab = ns * 4 + wv;
  constexpr size_t GS = 32768;
  const size_t goff = GS ? (size_t)mg * GS : (size_t)xcd * (128 * 32768) + (size_t)(jj >> 2) * 32768;
  const unsigned char* xb = (const unsigned char*)x + goff;
  unsigned fw; unsigned long long tag;
  unsigned voff[4];
#pragma unroll
  for (int r = 0; r < 4; ++r) voff[r] = (unsigned)((4 * wv + r) * 2048 + ((lane ^ (4 * wv + r)) * 16));
  {
    const int ps = bid >> 4, s = bid & 15;
    unsigned char* blob = ws + WS_BLOB + (size_t)ps * NREC * 1024;
    unsigned* rv = (unsigned*)(ws + WS_RV) + (size_t)ps * 4 * RV_ENT * 16;
    const int p = lane >> 4, q = lane & 15;
    const int k0 = 64 * s + 16 * wv + 4 * p;
    const int t = lane >> 4, rr = lane & 15;
    const int col = 32 * (t >> 1) + 8 * (rr >> 2) + 4 * (t & 1) + (rr & 3);
    const unsigned woff = (unsigned)((k0 * ND + ps * 64 + 4 * q) * 2);
    const unsigned boff = (unsigned)((ps * 64 + col) * 2);
    const unsigned m0b = (unsigned)(4 * wv * ROWB);
    u32x2 r[4], ep; unsigned bv, dummy; const unsigned zero = 0;
    asm volatile(
        "global_load_dwordx2 %0, %7, %8\n\t"
        "global_load_dwordx2 %1, %7, %8 offset:2048\n\t"
        "v_add_u32 %4, 4096, %7\n\t"
        "global_load_dwordx2 %2, %4, %8\n\t"
        "global_load_dwordx2 %3, %4, %8 offset:2048\n\t"
        "global_load_dwordx2 %6, %18, %19 sc1\n\t"
        "global_load_ushort %4, %9, %10\n\t"
        "s_add_u32 m0, %15, 0\n\t"
        "s_nop 0\n\t"
        "global_load_lds_dwordx4 %11, %16\n\t"
        "global_load_lds_dwordx4 %11, %16 offset:1024\n\t"
        "s_add_u32 m0, %15, 8192\n\t"
        "s_nop 0\n\t"
        "global_load_lds_dwordx4 %12, %16\n\t"
        "global_load_lds_dwordx4 %12, %16 offset:1024\n\t"
        "s_add_u32 m0, %15, 16384\n\t"
        "s_nop 0\n\t"
        "global_load_lds_dwordx4 %13, %16\n\t"
        "global_load_lds_dwordx4 %13, %16 offset:1024\n\t"
        "s_add_u32 m0, %15, 24576\n\t"
        "s_nop 0\n\t"
        "global_load_lds_dwordx4 %14, %16\n\t"
        "global_load_lds_dwordx4 %14, %16 offset:1024\n\t"
        "s_getpc_b64 s[98:99]\n\t"
        "s_add_u32 s98, s98, 2048\n\t"
        "s_addc_u32 s99, s99, 0\n\t"
        "global_load_dword %5, %17, s[98:99]\n\t"
        "s_add_u32 s98, s98, 4096\n\t"
        "s_addc_u32 s99, s99, 0\n\t"
        "global_load_dword %5, %17, s[98:99]\n\t"
        "s_add_u32 s98, s98, 4096\n\t"
        "s_addc_u32 s99, s99, 0\n\t"
        "global_load_dword %5, %17, s[98:99]\n\t"
        "s_add_u32 s98, s98, 4096\n\t"
        "s_addc_u32 s99, s99, 0\n\t"
        "s_waitcnt vmcnt(11)\n\t"
        : "=&v"(r[0]), "=&v"(r[1]), "=&v"(r[2]), "=&v"(r[3]), "=&v"(bv), "=&{v255}"(dummy), "=&v"(ep)
        : "v"(woff), "s"(w), "v"(boff), "s"(bias),
          "v"(voff[0]), "v"(voff[1]), "v"(voff[2]), "v"(voff[3]), "s"(m0b), "s"(xb), "v"(lane * 64), "v"(zero), "s"(ws + WS_EPOCH)
        : "memory", "scc", "s98", "s99");
    tag = (((unsigned long long)(unsigned)__builtin_amdgcn_readfirstlane(ep[1]) << 32) | (unsigned)__builtin_amdgcn_readfirstlane(ep[0])) + 1ull;
    unsigned char* scr = lds + LDS_SCR + wv * 2048;
    unsigned* LM = (unsigned*)scr;
    unsigned char* LI = scr + 1024;
    unsigned rm[4];
#pragma unroll
    for (int i = 0; i < 4; ++i) {
      const int sh = (i & 1) * 16;
      const unsigned a0 = mkkey((r[0][i >> 1] >> sh) & 0xffffu, 0), a1 = mkkey((r[1][i >> 1] >> sh) & 0xffffu, 1),
                     a2 = mkkey((r[2][i >> 1] >> sh) & 0xffffu, 2), a3 = mkkey((r[3][i >> 1] >> sh) & 0xffffu, 3);
      const unsigned a = max(a0, a1), b = min(a0, a1), c = max(a2, a3), d = min(a2, a3);
      const unsigned m1 = min(a, c), m2 = max(b, d);
      const unsigned s0 = max(a, c), s1 = max(m1, m2), s2 = min(m1, m2), s3 = min(b, d);
      LM[(4 * q + i) * 4 + p] = keyval(s0) | (keyval(s1) << 16);
      LI[(4 * q + i) * 4 + p] = (unsigned char)(keypos(s0) | (keypos(s1) << 2));
      const unsigned thr = s1 >> 3;
      rm[i] = 0;
      if (thr != 0 && (s2 >> 3) == thr) rm[i] |= 1u << keypos(s2);
      if (thr != 0 && (s3 >> 3) == thr) rm[i] |= 1u << keypos(s3);
    }
    __builtin_amdgcn_fence(__ATOMIC_RELEASE, "wavefront");
    __builtin_amdgcn_wave_barrier();
    __builtin_amdgcn_fence(__ATOMIC_ACQUIRE, "wavefront");
    const int al = wv * 16 + rr;
    st16(blob + ((s >> 1) * 9 + 1 + (s & 1) * 4 + t) * 1024 + al * 16, *(const u32x4*)(LM + col * 4));
    if (lane < 16) {
      unsigned i16[4];
#pragma unroll
      for (int tt = 0; tt < 4; ++tt) {
        const unsigned nb = *(const unsigned*)(LI + (32 * (tt >> 1) + 8 * (rr >> 2) + 4 * (tt & 1) + (rr & 3)) * 4);
        i16[tt] = (nb & 0xf) | ((nb >> 4) & 0xf0) | ((nb >> 8) & 0xf00) | ((nb >> 12) & 0xf000);
      }
      st8(blob + (s >> 1) * 9 * 1024 + (s & 1) * 512 + al * 8, u32x2{i16[0] | (i16[1] << 16), i16[2] | (i16[3] << 16)});
    }
    unsigned pm = rm[0] | rm[1] | rm[2] | rm[3];
    pm |= (unsigned)__builtin_amdgcn_update_dpp(0, (int)pm, 0x4E, 0xf, 0xf, false);
    pm |= (unsigned)__builtin_amdgcn_update_dpp(0, (int)pm, 0x104, 0xf, 0x5, false)
        | (unsigned)__builtin_amdgcn_update_dpp(0, (int)pm, 0x114, 0xf, 0xa, false);
    const int tq = 2 * (q >> 3) + (q & 1), gq = (q & 7) >> 1;
    if (pm) {
#pragma unroll
      for (int h = 0; h < 2; ++h) if ((pm >> (2 * h)) & 3) {
        u32x4 o;
#pragma unroll
        for (int i = 0; i < 4; ++i) {
          const int sh = (i & 1) * 16;
          const unsigned lo = (r[2 * h][i >> 1] >> sh) & 0xffffu, hi = (r[2 * h + 1][i >> 1] >> sh) & 0xffffu;
          o[i] = (((rm[i] >> (2 * h)) & 1) ? lo : 0u) | (((rm[i] >> (2 * h + 1)) & 1) ? (hi << 16) : 0u);
        }
        st16(rv + ((size_t)tq * RV_ENT + (k0 >> 1) + h) * 16 + 4 * gq, o);
      }
    }
    unsigned f = (((pm & 3) ? 1u : 0u) | ((pm & 12) ? 2u : 0u)) << (2 * p + 8 * tq);
    f |= (unsigned)__builtin_amdgcn_update_dpp(0, (int)f, 0x111, 0xf, 0xf, false);
    f |= (unsigned)__builtin_amdgcn_update_dpp(0, (int)f, 0x118, 0xf, 0xf, false);
    fw = __builtin_amdgcn_readlane(f, 9) | __builtin_amdgcn_readlane(f, 25) | __builtin_amdgcn_readlane(f, 41) | __builtin_amdgcn_readlane(f, 57);
    if (s == 0 && wv == 0) {
      st4(rv + ((size_t)t * RV_ENT + K_ZERO) * 16 + rr, 0u);
      st4(rv + ((size_t)t * RV_ENT + K_BIAS) * 16 + rr, bv);
    }
  }
  unsigned char* ob = (unsigned char*)out + goff + slab * 128;
  const unsigned char* blob = ws + WS_BLOB + (size_t)slab * NREC * 1024;
  const unsigned char* rv = ws + WS_RV + (size_t)slab * 4 * RV_ENT * 64;
  unsigned char* gl = ws + WS_GL + (size_t)(bid * 4 + wv) * 4096;
  asm volatile(
      "s_mov_b64 s[34:35], %15\n\t"
      "s_mov_b64 s[36:37], %2\n\t"
      "s_add_u32 s36, s36, 2097152\n\t"
      "s_addc_u32 s37, s37, 0\n\t"
      "s_lshl_b32 s46, %1, 15\n\t"
      "s_add_u32 s47, s46, 8192\n\t"
      "s_add_u32 s48, s46, 16384\n\t"
      "s_add_u32 s49, s46, 24576\n\t"
      "s_add_u32 m0, s46, 2048\n\t"
      "s_nop 0\n\t"
      "global_load_lds_dwordx4 %11, s[36:37]\n\t"
      "global_load_lds_dwordx4 %11, s[36:37] offset:1024\n\t"
      "s_add_u32 m0, s47, 2048\n\t"
      "s_nop 0\n\t"
      "global_load_lds_dwordx4 %12, s[36:37]\n\t"
      "global_load_lds_dwordx4 %12, s[36:37] offset:1024\n\t"
      "s_add_u32 m0, s48, 2048\n\t"
      "s_nop 0\n\t"
      "global_load_lds_dwordx4 %13, s[36:37]\n\t"
      "global_load_lds_dwordx4 %13, s[36:37] offset:1024\n\t"
      "s_add_u32 m0, s49, 2048\n\t"
      "s_nop 0\n\t"
      "global_load_lds_dwordx4 %14, s[36:37]\n\t"
      "global_load_lds_dwordx4 %14, s[36:37] offset:1024\n\t"
      "s_add_u32 s36, s36, 2097152\n\t"
      "s_addc_u32 s37, s37, 0\n\t"
      "s_mov_b64 s[60:61], %8\n\t"
      "s_add_u32 s60, s60, 4096\n\t"
      "s_addc_u32 s61, s61, 0\n\t"
      "v_mov_b32 v231, %9\n\t"
      "s_lshl_b32 s55, %1, 2\n\t"
      "s_add_u32 s55, s55, 139264\n\t"
      "v_mov_b32 v249, s55\n\t"
      "ds_write_b32 v249, v231\n\t"
      "v_lshlrev_b32 v250, 2, %0\n\t"
      "v_add_u32 v250, 139264, v250\n\t"
      "s_lshl_b32 s55, %10, 5\n\t"
      "v_lshl_add_u32 v249, %0, 3, s55\n\t"
      "v_mov_b32 v230, s34\n\t"
      "s_cmp_eq_u32 %1, 0\n\t"
      "s_waitcnt vmcnt(8) lgkmcnt(0)\n\t"
      "s_barrier\n\t"
      "s_cbranch_scc0 .Lnopub%=\n\t"
      "s_mov_b64 exec, 15\n\t"
      "ds_read_b32 v231, v250\n\t"
      "s_waitcnt lgkmcnt(0)\n\t"
      "global_store_dwordx2 v249, v[230:231], s[60:61] sc1\n\t"
      "s_mov_b64 exec, -1\n\t"
      ".Lnopub%=:\n\t"
      "s_add_u32 m0, s46, 4096\n\t"
      "s_nop 0\n\t"
      "global_load_lds_dwordx4 %11, s[36:37]\n\t"
      "global_load_lds_dwordx4 %11, s[36:37] offset:1024\n\t"
      "s_add_u32 m0, s47, 4096\n\t"
      "s_nop 0\n\t"
      "global_load_lds_dwordx4 %12, s[36:37]\n\t"
      "global_load_lds_dwordx4 %12, s[36:37] offset:1024\n\t"
      "s_add_u32 m0, s48, 4096\n\t"
      "s_nop 0\n\t"
      "global_load_lds_dwordx4 %13, s[36:37]\n\t"
      "global_load_lds_dwordx4 %13, s[36:37] offset:1024\n\t"
      "s_add_u32 m0, s49, 4096\n\t"
      "s_nop 0\n\t"
      "global_load_lds_dwordx4 %14, s[36:37]\n\t"
      "global_load_lds_dwordx4 %14, s[36:37] offset:1024\n\t"
      "s_add_u32 s36, s36, 2097152\n\t"
      "s_addc_u32 s37, s37, 0\n\t"
      "s_add_u32 m0, s46, 6144\n\t"
      "s_nop 0\n\t"
      "global_load_lds_dwordx4 %11, s[36:37]\n\t"
      "global_load_lds_dwordx4 %11, s[36:37] offset:1024\n\t"
      "s_add_u32 m0, s47, 6144\n\t"
      "s_nop 0\n\t"
      "global_load_lds_dwordx4 %12, s[36:37]\n\t"
      "global_load_lds_dwordx4 %12, s[36:37] offset:1024\n\t"
      "s_add_u32 m0, s48, 6144\n\t"
      "s_nop 0\n\t"
      "global_load_lds_dwordx4 %13, s[36:37]\n\t"
      "global_load_lds_dwordx4 %13, s[36:37] offset:1024\n\t"
      "s_add_u32 m0, s49, 6144\n\t"
      "s_nop 0\n\t"
      "global_load_lds_dwordx4 %14, s[36:37]\n\t"
      "global_load_lds_dwordx4 %14, s[36:37] offset:1024\n\t"
      "s_add_u32 s36, s36, 2097152\n\t"
      "s_addc_u32 s37, s37, 0\n\t"
      "v_mov_b32 v38, 0\n\t"
      "v_mov_b32 v39, 0\n\t"
      "v_mov_b32 v40, 0\n\t"
      "v_mov_b32 v41, 0\n\t"
      "v_mov_b32 v42, 0\n\t"
      "v_mov_b32 v43, 0\n\t"
      "v_mov_b32 v44, 0\n\t"
      "v_mov_b32 v45, 0\n\t"
      "v_mov_b32 v46, 0\n\t"
      "v_mov_b32 v47, 0\n\t"
      "v_mov_b32 v48, 0\n\t"
      "v_mov_b32 v49, 0\n\t"
      "v_mov_b32 v50, 0\n\t"
      "v_mov_b32 v51, 0\n\t"
      "v_mov_b32 v52, 0\n\t"
      "v_mov_b32 v53, 0\n\t"
      "v_mov_b32 v54, 0\n\t"
      "v_mov_b32 v55, 0\n\t"
      "v_mov_b32 v56, 0\n\t"
      "v_mov_b32 v57, 0\n\t"
      "v_mov_b32 v58, 0\n\t"
      "v_mov_b32 v59, 0\n\t"
      "v_mov_b32 v60, 0\n\t"
      "v_mov_b32 v61, 0\n\t"
      "v_mov_b32 v62, 0\n\t"
      "v_mov_b32 v63, 0\n\t"
      "v_mov_b32 v64, 0\n\t"
      "v_mov_b32 v65, 0\n\t"
      "v_mov_b32 v66, 0\n\t"
      "v_mov_b32 v67, 0\n\t"
      "v_mov_b32 v68, 0\n\t"
      "v_mov_b32 v69, 0\n\t"
      "s_mov_b64 s[40:41], %4\n\t"
      "s_mov_b64 s[38:39], %3\n\t"
      "s_mov_b64 s[42:43], %5\n\t"
      "s_mov_b64 s[44:45], %6\n\t"
      "v_lshlrev_b32 v245, 4, %0\n\t"
      "v_and_b32 v249, 15, %0\n\t"
      "v_lshrrev_b32 v248, 4, %0\n\t"
      "v_mul_u32_u24 v246, 8208, v249\n\t"
      "v_lshlrev_b32 v247, 2, v249\n\t"
      "v_lshlrev_b32 v250, 4, v248\n\t"
      "v_xor_b32 v238, v250, v246\n\t"
      "v_xor_b32 v239, 64, v238\n\t"
      "v_xor_b32 v240, 128, v238\n\t"
      "v_xor_b32 v241, 192, v238\n\t"
      "v_lshl_add_u32 v244, v249, 11, v250\n\t"
      "s_mov_b32 s68, -1\n\t"
      "s_mov_b32 s69, 0xffff\n\t"
      "s_movk_i32 s58, 64\n\t"
      "s_lshl_b32 s70, %1, 11\n\t"
      "s_add_u32 s70, s70, 131072\n\t"
      "v_mov_b32 v214, 33554432\n\t"
      "v_mov_b32 v215, 33554432\n\t"
      "v_mov_b32 v216, 33554432\n\t"
      "v_mov_b32 v217, 33554432\n\t"
      "v_cmp_eq_u32 vcc, 12, v249\n\t"
      "v_mov_b32 v250, 33619968\n\t"
      "v_add_u32 v249, s70, v245\n\t"
      "v_cndmask_b32 v217, v217, v250, vcc\n\t"
      "ds_write_b128 v249, v[214:217]\n\t"
      "v_lshlrev_b32 v233, 3, %0\n\t"
      "v_lshlrev_b32 v252, 3, %0\n\t"
      "s_lshl_b32 s55, %7, 9\n\t"
      "v_add_u32 v249, s55, v233\n\t"
      "s_mov_b32 s56, 200000\n\t"
      ".Lpoll%=:\n\t"
      "global_load_dwordx2 v[242:243], v249, s[60:61] sc1\n\t"
      "s_waitcnt vmcnt(0)\n\t"
      "v_cmp_ne_u32 vcc, s34, v242\n\t"
      "s_cbranch_vccz .Lpolled%=\n\t"
      "s_sleep 1\n\t"
      "s_sub_u32 s56, s56, 1\n\t"
      "s_cmp_lg_u32 s56, 0\n\t"
      "s_cbranch_scc1 .Lpoll%=\n\t"
      ".Lpolled%=:\n\t"
      "v_mov_b32 v232, v243\n\t"
      "s_movk_i32 s57, 0x1000\n\t"
      "s_mov_b32 s66, 1\n\t"
      "s_movk_i32 s67, 0x1000\n\t"
      "s_mov_b32 s72, 2097152\n\t"
      "s_mov_b32 s73, 2097152\n\t"
      "global_load_dwordx2 v[6:7], v252, s[40:41] sc1\n\t"
      "global_load_dwordx2 v[8:9], v252, s[40:41] offset:512 sc1\n\t"
      "global_load_dwordx4 a[0:3], v245, s[40:41] offset:1024 sc1\n\t"
      "global_load_dwordx4 a[4:7], v245, s[40:41] offset:2048 sc1\n\t"
      "global_load_dwordx4 a[8:11], v245, s[40:41] offset:3072 sc1\n\t"
      "s_add_u32 s40, s40, 4096\n\t"
      "s_addc_u32 s41, s41, 0\n\t"
      "global_load_dwordx4 a[12:15], v245, s[40:41] sc1\n\t"
      "global_load_dwordx4 a[16:19], v245, s[40:41] offset:1024 sc1\n\t"
      "global_load_dwordx4 a[20:23], v245, s[40:41] offset:2048 sc1\n\t"
      "global_load_dwordx4 a[24:27], v245, s[40:41] offset:3072 sc1\n\t"
      "s_add_u32 s40, s40, 4096\n\t"
      "s_addc_u32 s41, s41, 0\n\t"
      "global_load_dwordx4 a[28:31], v245, s[40:41] sc1\n\t"
      "global_load_dwordx2 v[10:11], v252, s[40:41] offset:1024 sc1\n\t"
      "global_load_dwordx2 v[12:13], v252, s[40:41] offset:1536 sc1\n\t"
      "global_load_dwordx4 a[32:35], v245, s[40:41] offset:2048 sc1\n\t"
      "global_load_dwordx4 a[36:39], v245, s[40:41] offset:3072 sc1\n\t"
      "s_add_u32 s40, s40, 4096\n\t"
      "s_addc_u32 s41, s41, 0\n\t"
      "global_load_dwordx4 a[40:43], v245, s[40:41] sc1\n\t"
      "global_load_dwordx4 a[44:47], v245, s[40:41] offset:1024 sc1\n\t"
      "global_load_dwordx4 a[48:51], v245, s[40:41] offset:2048 sc1\n\t"
      "global_load_dwordx4 a[52:55], v245, s[40:41] offset:3072 sc1\n\t"
      "s_add_u32 s40, s40, 4096\n\t"
      "s_addc_u32 s41, s41, 0\n\t"
      "global_load_dwordx4 a[56:59], v245, s[40:41] sc1\n\t"
      "global_load_dwordx4 a[60:63], v245, s[40:41] offset:1024 sc1\n\t"
      "s_barrier\n\t"
      "s_andn2_b32 s55, %10, 24\n\t"
      "s_or_b32 s55, s55, %1\n\t"
      "s_cmp_eq_u32 s55, 0\n\t"
      "s_cbranch_scc1 .Lseen%=\n\t"
      ".Lseenr%=:\n\t"
      "s_waitcnt vmcnt(10)\n\t"
      "ds_read_b128 v[86:89], v238 offset:0\n\t"
      "ds_read_b128 v[90:93], v239 offset:0\n\t"
      "ds_read_b128 v[94:97], v240 offset:0\n\t"
      "ds_read_b128 v[98:101], v241 offset:0\n\t"
      "s_waitcnt lgkmcnt(0)\n\t"
      "v_smfmac_f32_16x16x64_bf16 v[38:41], a[0:3], v[86:93], v6\n\t"
      "ds_read_b128 v[102:105], v238 offset:2048\n\t"
      "global_load_dwordx2 v[14:15], v252, s[40:41] offset:2048 sc1\n\t"
      "global_load_dwordx2 v[16:17], v252, s[40:41] offset:2560 sc1\n\t"
      "v_smfmac_f32_16x16x64_bf16 v[38:41], a[16:19], v[94:101], v8\n\t"
      "ds_read_b128 v[106:109], v239 offset:2048\n\t"
      "v_smfmac_f32_16x16x64_bf16 v[42:45], a[4:7], v[86:93], v6 abid:1\n\t"
      "ds_read_b128 v[110:113], v240 offset:2048\n\t"
      "v_smfmac_f32_16x16x64_bf16 v[42:45], a[20:23], v[94:101], v8 abid:1\n\t"
      "ds_read_b128 v[114:117], v241 offset:2048\n\t"
      "global_load_dwordx4 a[64:67], v245, s[40:41] offset:3072 sc1\n\t"
      "v_smfmac_f32_16x16x64_bf16 v[46:49], a[8:11], v[86:93], v7\n\t"
      "ds_read_b128 v[70:73], v238 offset:256\n\t"
      "v_smfmac_f32_16x16x64_bf16 v[46:49], a[24:27], v[94:101], v9\n\t"
      "ds_read_b128 v[74:77], v239 offset:256\n\t"
      "v_smfmac_f32_16x16x64_bf16 v[50:53], a[12:15], v[86:93], v7 abid:1\n\t"
      "ds_read_b128 v[78:81], v240 offset:256\n\t"
      "s_add_u32 s40, s40, 4096\n\t"
      "s_addc_u32 s41, s41, 0\n\t"
      "global_load_dwordx4 a[68:71], v245, s[40:41] sc1\n\t"
      "v_smfmac_f32_16x16x64_bf16 v[50:53], a[28:31], v[94:101], v9 abid:1\n\t"
      "ds_read_b128 v[82:85], v241 offset:256\n\t"
      "s_waitcnt lgkmcnt(4)\n\t"
      "v_smfmac_f32_16x16x64_bf16 v[54:57], a[0:3], v[102:109], v6\n\t"
      "ds_read_b128 v[86:89], v238 offset:2304\n\t"
      "v_smfmac_f32_16x16x64_bf16 v[54:57], a[16:19], v[110:117], v8\n\t"
      "ds_read_b128 v[90:93], v239 offset:2304\n\t"
      "global_load_dwordx4 a[72:75], v245, s[40:41] offset:1024 sc1\n\t"
      "v_smfmac_f32_16x16x64_bf16 v[58:61], a[4:7], v[102:109], v6 abid:1\n\t"
      "ds_read_b128 v[94:97], v240 offset:2304\n\t"
      "v_smfmac_f32_16x16x64_bf16 v[58:61], a[20:23], v[110:117], v8 abid:1\n\t"
      "ds_read_b128 v[98:101], v241 offset:2304\n\t"
      "v_smfmac_f32_16x16x64_bf16 v[62:65], a[8:11], v[102:109], v7\n\t"
      "global_load_dwordx4 a[76:79], v245, s[40:41] offset:2048 sc1\n\t"
      "v_smfmac_f32_16x16x64_bf16 v[62:65], a[24:27], v[110:117], v9\n\t"
      "v_smfmac_f32_16x16x64_bf16 v[66:69], a[12:15], v[102:109], v7 abid:1\n\t"
      "v_smfmac_f32_16x16x64_bf16 v[66:69], a[28:31], v[110:117], v9 abid:1\n\t"
      "global_load_dwordx4 a[80:83], v245, s[40:41] offset:3072 sc1\n\t"
      "v_mov_b32 v217, 0x3f803f80\n\t"
      "v_mov_b32 v221, 0x3f803f80\n\t"
      "v_mov_b32 v225, 0x3f803f80\n\t"
      "v_mov_b32 v229, 0x3f803f80\n\t"
      "v_bfe_u32 v249, v232, 0, 8\n\t"
      "s_mov_b32 s50, 0\n\t"
      "v_cmp_ne_u32 vcc, 0, v249\n\t"
      "s_cbranch_vccz .Lcd0_%=\n\t"
      ".Lc0_%=:\n\t"
      "s_mov_b64 exec, vcc\n\t"
      "v_ffbl_b32 v251, v249\n\t"
      "v_mbcnt_lo_u32_b32 v250, vcc_lo, 0\n\t"
      "v_mbcnt_hi_u32_b32 v250, vcc_hi, v250\n\t"
      "v_add_u32 v250, s50, v250\n\t"
      "s_bcnt1_i32_b64 s55, vcc\n\t"
      "s_add_u32 s50, s50, s55\n\t"
      "v_cmp_lt_u32 vcc, 14, v250\n\t"
      "v_add_u32 v251, v251, v233\n\t"
      "v_add_u32 v231, -1, v249\n\t"
      "v_addc_co_u32 v250, vcc, 0, v250, vcc\n\t"
      "v_and_b32 v249, v249, v231\n\t"
      "v_mul_u32_u24 v230, 65540, v251\n\t"
      "s_cmp_ge_u32 s50, 40\n\t"
      "s_cbranch_scc1 .Lco0_%=\n\t"
      ".Lcr0_%=:\n\t"
      "v_and_b32 v231, 3, v250\n\t"
      "v_and_b32 v250, -4, v250\n\t"
      "v_lshl_add_u32 v231, v231, 6, v250\n\t"
      "v_add_u32 v231, s70, v231\n\t"
      "ds_write_b32 v231, v230 offset:0\n\t"
      ".Lcs0_%=:\n\t"
      "s_mov_b64 exec, -1\n\t"
      "v_cmp_ne_u32 vcc, 0, v249\n\t"
      "s_cbranch_vccnz .Lc0_%=\n\t"
      ".Lcd0_%=:\n\t"
      "s_cmp_gt_u32 s50, 15\n\t"
      "s_addc_u32 s50, s50, 0\n\t"
      "s_add_u32 s40, s40, 4096\n\t"
      "s_addc_u32 s41, s41, 0\n\t"
      "global_load_dwordx4 a[84:87], v245, s[40:41] sc1\n\t"
      "global_load_dwordx4 a[88:91], v245, s[40:41] offset:1024 sc1\n\t"
      "global_load_dwordx4 a[92:95], v245, s[40:41] offset:2048 sc1\n\t"
      "global_load_dwordx2 v[18:19], v252, s[40:41] offset:3072 sc1\n\t"
      "global_load_dwordx2 v[20:21], v252, s[40:41] offset:3584 sc1\n\t"
      "v_bfe_u32 v249, v232, 8, 8\n\t"
      "s_mov_b32 s51, 0\n\t"
      "v_cmp_ne_u32 vcc, 0, v249\n\t"
      "s_cbranch_vccz .Lcd1_%=\n\t"
      ".Lc1_%=:\n\t"
      "s_mov_b64 exec, vcc\n\t"
      "v_ffbl_b32 v251, v249\n\t"
      "v_mbcnt_lo_u32_b32 v250, vcc_lo, 0\n\t"
      "v_mbcnt_hi_u32_b32 v250, vcc_hi, v250\n\t"
      "v_add_u32 v250, s51, v250\n\t"
      "s_bcnt1_i32_b64 s55, vcc\n\t"
      "s_add_u32 s51, s51, s55\n\t"
      "v_cmp_lt_u32 vcc, 14, v250\n\t"
      "v_add_u32 v251, v251, v233\n\t"
      "v_add_u32 v231, -1, v249\n\t"
      "v_addc_co_u32 v250, vcc, 0, v250, vcc\n\t"
      "v_and_b32 v249, v249, v231\n\t"
      "v_mul_u32_u24 v230, 65540, v251\n\t"
      "s_cmp_ge_u32 s51, 40\n\t"
      "s_cbranch_scc1 .Lco1_%=\n\t"
      ".Lcr1_%=:\n\t"
      "v_and_b32 v231, 3, v250\n\t"
      "v_and_b32 v250, -4, v250\n\t"
      "v_lshl_add_u32 v231, v231, 6, v250\n\t"
      "v_add_u32 v231, s70, v231\n\t"
      "ds_write_b32 v231, v230 offset:256\n\t"
      ".Lcs1_%=:\n\t"
      "s_mov_b64 exec, -1\n\t"
      "v_cmp_ne_u32 vcc, 0, v249\n\t"
      "s_cbranch_vccnz .Lc1_%=\n\t"
      ".Lcd1_%=:\n\t"
      "s_cmp_gt_u32 s51, 15\n\t"
      "s_addc_u32 s51, s51, 0\n\t"
      "s_add_u32 s40, s40, 4096\n\t"
      "s_addc_u32 s41, s41, 0\n\t"
      "global_load_dwordx4 a[96:99], v245, s[40:41] sc1\n\t"
      "global_load_dwordx4 a[100:103], v245, s[40:41] offset:1024 sc1\n\t"
      "global_load_dwordx4 a[104:107], v245, s[40:41] offset:2048 sc1\n\t"
      "global_load_dwordx4 a[108:111], v245, s[40:41] offset:3072 sc1\n\t"
      "v_bfe_u32 v249, v232, 16, 8\n\t"
      "s_mov_b32 s52, 0\n\t"
      "v_cmp_ne_u32 vcc, 0, v249\n\t"
      "s_cbranch_vccz .Lcd2_%=\n\t"
      ".Lc2_%=:\n\t"
      "s_mov_b64 exec, vcc\n\t"
      "v_ffbl_b32 v251, v249\n\t"
      "v_mbcnt_lo_u32_b32 v250, vcc_lo, 0\n\t"
      "v_mbcnt_hi_u32_b32 v250, vcc_hi, v250\n\t"
      "v_add_u32 v250, s52, v250\n\t"
      "s_bcnt1_i32_b64 s55, vcc\n\t"
      "s_add_u32 s52, s52, s55\n\t"
      "v_cmp_lt_u32 vcc, 14, v250\n\t"
      "v_add_u32 v251, v251, v233\n\t"
      "v_add_u32 v231, -1, v249\n\t"
      "v_addc_co_u32 v250, vcc, 0, v250, vcc\n\t"
      "v_and_b32 v249, v249, v231\n\t"
      "v_mul_u32_u24 v230, 65540, v251\n\t"
      "s_cmp_ge_u32 s52, 40\n\t"
      "s_cbranch_scc1 .Lco2_%=\n\t"
      ".Lcr2_%=:\n\t"
      "v_and_b32 v231, 3, v250\n\t"
      "v_and_b32 v250, -4, v250\n\t"
      "v_lshl_add_u32 v231, v231, 6, v250\n\t"
      "v_add_u32 v231, s70, v231\n\t"
      "ds_write_b32 v231, v230 offset:512\n\t"
      ".Lcs2_%=:\n\t"
      "s_mov_b64 exec, -1\n\t"
      "v_cmp_ne_u32 vcc, 0, v249\n\t"
      "s_cbranch_vccnz .Lc2_%=\n\t"
      ".Lcd2_%=:\n\t"
      "s_cmp_gt_u32 s52, 15\n\t"
      "s_addc_u32 s52, s52, 0\n\t"
      "s_add_u32 s40, s40, 4096\n\t"
      "s_addc_u32 s41, s41, 0\n\t"
      "global_load_dwordx4 a[112:115], v245, s[40:41] sc1\n\t"
      "global_load_dwordx4 a[116:119], v245, s[40:41] offset:1024 sc1\n\t"
      "global_load_dwordx4 a[120:123], v245, s[40:41] offset:2048 sc1\n\t"
      "global_load_dwordx4 a[124:127], v245, s[40:41] offset:3072 sc1\n\t"
      "v_bfe_u32 v249, v232, 24, 8\n\t"
      "s_mov_b32 s53, 0\n\t"
      "v_cmp_ne_u32 vcc, 0, v249\n\t"
      "s_cbranch_vccz .Lcd3_%=\n\t"
      ".Lc3_%=:\n\t"
      "s_mov_b64 exec, vcc\n\t"
      "v_ffbl_b32 v251, v249\n\t"
      "v_mbcnt_lo_u32_b32 v250, vcc_lo, 0\n\t"
      "v_mbcnt_hi_u32_b32 v250, vcc_hi, v250\n\t"
      "v_add_u32 v250, s53, v250\n\t"
      "s_bcnt1_i32_b64 s55, vcc\n\t"
      "s_add_u32 s53, s53, s55\n\t"
      "v_cmp_lt_u32 vcc, 14, v250\n\t"
      "v_add_u32 v251, v251, v233\n\t"
      "v_add_u32 v231, -1, v249\n\t"
      "v_addc_co_u32 v250, vcc, 0, v250, vcc\n\t"
      "v_and_b32 v249, v249, v231\n\t"
      "v_mul_u32_u24 v230, 65540, v251\n\t"
      "s_cmp_ge_u32 s53, 40\n\t"
      "s_cbranch_scc1 .Lco3_%=\n\t"
      ".Lcr3_%=:\n\t"
      "v_and_b32 v231, 3, v250\n\t"
      "v_and_b32 v250, -4, v250\n\t"
      "v_lshl_add_u32 v231, v231, 6, v250\n\t"
      "v_add_u32 v231, s70, v231\n\t"
      "ds_write_b32 v231, v230 offset:768\n\t"
      ".Lcs3_%=:\n\t"
      "s_mov_b64 exec, -1\n\t"
      "v_cmp_ne_u32 vcc, 0, v249\n\t"
      "s_cbranch_vccnz .Lc3_%=\n\t"
      ".Lcd3_%=:\n\t"
      "s_cmp_gt_u32 s53, 15\n\t"
      "s_addc_u32 s53, s53, 0\n\t"
      "s_add_u32 s40, s40, 4096\n\t"
      "s_addc_u32 s41, s41, 0\n\t"
      "global_load_dwordx2 v[22:23], v252, s[40:41] sc1\n\t"
      "global_load_dwordx2 v[24:25], v252, s[40:41] offset:512 sc1\n\t"
      "global_load_dwordx4 a[128:131], v245, s[40:41] offset:1024 sc1\n\t"
      "global_load_dwordx4 a[132:135], v245, s[40:41] offset:2048 sc1\n\t"
      "global_load_dwordx4 a[136:139], v245, s[40:41] offset:3072 sc1\n\t"
      "s_max_u32 s55, s50, s51\n\t"
      "s_max_u32 s56, s52, s53\n\t"
      "s_max_u32 s54, s55, s56\n\t"
      "v_lshlrev_b32 v249, 6, v248\n\t"
      "v_add_u32 v249, s70, v249\n\t"
      "s_waitcnt lgkmcnt(0)\n\t"
      "ds_read_b128 v[134:137], v249 offset:0\n\t"
      "ds_read_b128 v[150:153], v249 offset:16\n\t"
      "ds_read_b64 v[198:199], v249 offset:32\n\t"
      "ds_read_b128 v[138:141], v249 offset:256\n\t"
      "ds_read_b128 v[154:157], v249 offset:272\n\t"
      "ds_read_b64 v[200:201], v249 offset:288\n\t"
      "ds_read_b128 v[142:145], v249 offset:512\n\t"
      "ds_read_b128 v[158:161], v249 offset:528\n\t"
      "ds_read_b64 v[202:203], v249 offset:544\n\t"
      "ds_read_b128 v[146:149], v249 offset:768\n\t"
      "ds_read_b128 v[162:165], v249 offset:784\n\t"
      "ds_read_b64 v[204:205], v249 offset:800\n\t"
      "v_add_u32 v250, 0, v247\n\t"
      "s_waitcnt lgkmcnt(9)\n\t"
      "v_xor_b32_sdwa v166, v246, v134 dst_sel:DWORD dst_unused:UNUSED_PAD src0_sel:DWORD src1_sel:WORD_0\n\t"
      "v_mad_u32_u16 v134, v134, s58, v250 op_sel:[1,0,0,0]\n\t"
      "global_load_dword v134, v134, s[42:43] sc1\n\t"
      "v_xor_b32_sdwa v167, v246, v135 dst_sel:DWORD dst_unused:UNUSED_PAD src0_sel:DWORD src1_sel:WORD_0\n\t"
      "v_mad_u32_u16 v135, v135, s58, v250 op_sel:[1,0,0,0]\n\t"
      "global_load_dword v135, v135, s[42:43] sc1\n\t"
      "v_xor_b32_sdwa v168, v246, v136 dst_sel:DWORD dst_unused:UNUSED_PAD src0_sel:DWORD src1_sel:WORD_0\n\t"
      "v_mad_u32_u16 v136, v136, s58, v250 op_sel:[1,0,0,0]\n\t"
      "global_load_dword v136, v136, s[42:43] sc1\n\t"
      "v_xor_b32_sdwa v169, v246, v137 dst_sel:DWORD dst_unused:UNUSED_PAD src0_sel:DWORD src1_sel:WORD_0\n\t"
      "v_mad_u32_u16 v137, v137, s58, v250 op_sel:[1,0,0,0]\n\t"
      "global_load_dword v137, v137, s[42:43] sc1\n\t"
      "v_xor_b32_sdwa v182, v246, v150 dst_sel:DWORD dst_unused:UNUSED_PAD src0_sel:DWORD src1_sel:WORD_0\n\t"
      "v_mad_u32_u16 v150, v150, s58, v250 op_sel:[1,0,0,0]\n\t"
      "global_load_dword v150, v150, s[42:43] sc1\n\t"
      "v_xor_b32_sdwa v183, v246, v151 dst_sel:DWORD dst_unused:UNUSED_PAD src0_sel:DWORD src1_sel:WORD_0\n\t"
      "v_mad_u32_u16 v151, v151, s58, v250 op_sel:[1,0,0,0]\n\t"
      "global_load_dword v151, v151, s[42:43] sc1\n\t"
      "v_xor_b32_sdwa v184, v246, v152 dst_sel:DWORD dst_unused:UNUSED_PAD src0_sel:DWORD src1_sel:WORD_0\n\t"
      "v_mad_u32_u16 v152, v152, s58, v250 op_sel:[1,0,0,0]\n\t"
      "global_load_dword v152, v152, s[42:43] sc1\n\t"
      "v_xor_b32_sdwa v185, v246, v153 dst_sel:DWORD dst_unused:UNUSED_PAD src0_sel:DWORD src1_sel:WORD_0\n\t"
      "v_mad_u32_u16 v153, v153, s58, v250 op_sel:[1,0,0,0]\n\t"
      "global_load_dword v153, v153, s[42:43] sc1\n\t"
      "v_add_u32 v250, 32896, v247\n\t"
      "s_waitcnt lgkmcnt(6)\n\t"
      "v_xor_b32_sdwa v170, v246, v138 dst_sel:DWORD dst_unused:UNUSED_PAD src0_sel:DWORD src1_sel:WORD_0\n\t"
      "v_mad_u32_u16 v138, v138, s58, v250 op_sel:[1,0,0,0]\n\t"
      "global_load_dword v138, v138, s[42:43] sc1\n\t"
      "v_xor_b32_sdwa v171, v246, v139 dst_sel:DWORD dst_unused:UNUSED_PAD src0_sel:DWORD src1_sel:WORD_0\n\t"
      "v_mad_u32_u16 v139, v139, s58, v250 op_sel:[1,0,0,0]\n\t"
      "global_load_dword v139, v139, s[42:43] sc1\n\t"
      "v_xor_b32_sdwa v172, v246, v140 dst_sel:DWORD dst_unused:UNUSED_PAD src0_sel:DWORD src1_sel:WORD_0\n\t"
      "v_mad_u32_u16 v140, v140, s58, v250 op_sel:[1,0,0,0]\n\t"
      "global_load_dword v140, v140, s[42:43] sc1\n\t"
      "v_xor_b32_sdwa v173, v246, v141 dst_sel:DWORD dst_unused:UNUSED_PAD src0_sel:DWORD src1_sel:WORD_0\n\t"
      "v_mad_u32_u16 v141, v141, s58, v250 op_sel:[1,0,0,0]\n\t"
      "global_load_dword v141, v141, s[42:43] sc1\n\t"
      "v_xor_b32_sdwa v186, v246, v154 dst_sel:DWORD dst_unused:UNUSED_PAD src0_sel:DWORD src1_sel:WORD_0\n\t"
      "v_mad_u32_u16 v154, v154, s58, v250 op_sel:[1,0,0,0]\n\t"
      "global_load_dword v154, v154, s[42:43] sc1\n\t"
      "v_xor_b32_sdwa v187, v246, v155 dst_sel:DWORD dst_unused:UNUSED_PAD src0_sel:DWORD src1_sel:WORD_0\n\t"
      "v_mad_u32_u16 v155, v155, s58, v250 op_sel:[1,0,0,0]\n\t"
      "global_load_dword v155, v155, s[42:43] sc1\n\t"
      "v_xor_b32_sdwa v188, v246, v156 dst_sel:DWORD dst_unused:UNUSED_PAD src0_sel:DWORD src1_sel:WORD_0\n\t"
      "v_mad_u32_u16 v156, v156, s58, v250 op_sel:[1,0,0,0]\n\t"
      "global_load_dword v156, v156, s[42:43] sc1\n\t"
      "v_xor_b32_sdwa v189, v246, v157 dst_sel:DWORD dst_unused:UNUSED_PAD src0_sel:DWORD src1_sel:WORD_0\n\t"
      "v_mad_u32_u16 v157, v157, s58, v250 op_sel:[1,0,0,0]\n\t"
      "global_load_dword v157, v157, s[42:43] sc1\n\t"
      "v_add_u32 v250, 65792, v247\n\t"
      "s_waitcnt lgkmcnt(3)\n\t"
      "v_xor_b32_sdwa v174, v246, v142 dst_sel:DWORD dst_unused:UNUSED_PAD src0_sel:DWORD src1_sel:WORD_0\n\t"
      "v_mad_u32_u16 v142, v142, s58, v250 op_sel:[1,0,0,0]\n\t"
      "global_load_dword v142, v142, s[42:43] sc1\n\t"
      "v_xor_b32_sdwa v175, v246, v143 dst_sel:DWORD dst_unused:UNUSED_PAD src0_sel:DWORD src1_sel:WORD_0\n\t"
      "v_mad_u32_u16 v143, v143, s58, v250 op_sel:[1,0,0,0]\n\t"
      "global_load_dword v143, v143, s[42:43] sc1\n\t"
      "v_xor_b32_sdwa v176, v246, v144 dst_sel:DWORD dst_unused:UNUSED_PAD src0_sel:DWORD src1_sel:WORD_0\n\t"
      "v_mad_u32_u16 v144, v144, s58, v250 op_sel:[1,0,0,0]\n\t"
      "global_load_dword v144, v144, s[42:43] sc1\n\t"
      "v_xor_b32_sdwa v177, v246, v145 dst_sel:DWORD dst_unused:UNUSED_PAD src0_sel:DWORD src1_sel:WORD_0\n\t"
      "v_mad_u32_u16 v145, v145, s58, v250 op_sel:[1,0,0,0]\n\t"
      "global_load_dword v145, v145, s[42:43] sc1\n\t"
      "v_xor_b32_sdwa v190, v246, v158 dst_sel:DWORD dst_unused:UNUSED_PAD src0_sel:DWORD src1_sel:WORD_0\n\t"
      "v_mad_u32_u16 v158, v158, s58, v250 op_sel:[1,0,0,0]\n\t"
      "global_load_dword v158, v158, s[42:43] sc1\n\t"
      "v_xor_b32_sdwa v191, v246, v159 dst_sel:DWORD dst_unused:UNUSED_PAD src0_sel:DWORD src1_sel:WORD_0\n\t"
      "v_mad_u32_u16 v159, v159, s58, v250 op_sel:[1,0,0,0]\n\t"
      "global_load_dword v159, v159, s[42:43] sc1\n\t"
      "v_xor_b32_sdwa v192, v246, v160 dst_sel:DWORD dst_unused:UNUSED_PAD src0_sel:DWORD src1_sel:WORD_0\n\t"
      "v_mad_u32_u16 v160, v160, s58, v250 op_sel:[1,0,0,0]\n\t"
      "global_load_dword v160, v160, s[42:43] sc1\n\t"
      "v_xor_b32_sdwa v193, v246, v161 dst_sel:DWORD dst_unused:UNUSED_PAD src0_sel:DWORD src1_sel:WORD_0\n\t"
      "v_mad_u32_u16 v161, v161, s58, v250 op_sel:[1,0,0,0]\n\t"
      "global_load_dword v161, v161, s[42:43] sc1\n\t"
      "v_add_u32 v250, 98688, v247\n\t"
      "s_waitcnt lgkmcnt(0)\n\t"
      "v_xor_b32_sdwa v178, v246, v146 dst_sel:DWORD dst_unused:UNUSED_PAD src0_sel:DWORD src1_sel:WORD_0\n\t"
      "v_mad_u32_u16 v146, v146, s58, v250 op_sel:[1,0,0,0]\n\t"
      "global_load_dword v146, v146, s[42:43] sc1\n\t"
      "v_xor_b32_sdwa v179, v246, v147 dst_sel:DWORD dst_unused:UNUSED_PAD src0_sel:DWORD src1_sel:WORD_0\n\t"
      "v_mad_u32_u16 v147, v147, s58, v250 op_sel:[1,0,0,0]\n\t"
      "global_load_dword v147, v147, s[42:43] sc1\n\t"
      "v_xor_b32_sdwa v180, v246, v148 dst_sel:DWORD dst_unused:UNUSED_PAD src0_sel:DWORD src1_sel:WORD_0\n\t"
      "v_mad_u32_u16 v148, v148, s58, v250 op_sel:[1,0,0,0]\n\t"
      "global_load_dword v148, v148, s[42:43] sc1\n\t"
      "v_xor_b32_sdwa v181, v246, v149 dst_sel:DWORD dst_unused:UNUSED_PAD src0_sel:DWORD src1_sel:WORD_0\n\t"
      "v_mad_u32_u16 v149, v149, s58, v250 op_sel:[1,0,0,0]\n\t"
      "s_waitcnt vmcnt(61)\n\t"
      "global_load_dword v149, v149, s[42:43] sc1\n\t"
      "v_xor_b32_sdwa v194, v246, v162 dst_sel:DWORD dst_unused:UNUSED_PAD src0_sel:DWORD src1_sel:WORD_0\n\t"
      "v_mad_u32_u16 v162, v162, s58, v250 op_sel:[1,0,0,0]\n\t"
      "s_waitcnt vmcnt(61)\n\t"
      "global_load_dword v162, v162, s[42:43] sc1\n\t"
      "v_xor_b32_sdwa v195, v246, v163 dst_sel:DWORD dst_unused:UNUSED_PAD src0_sel:DWORD src1_sel:WORD_0\n\t"
      "v_mad_u32_u16 v163, v163, s58, v250 op_sel:[1,0,0,0]\n\t"
      "s_waitcnt vmcnt(61)\n\t"
      "global_load_dword v163, v163, s[42:43] sc1\n\t"
      "v_xor_b32_sdwa v196, v246, v164 dst_sel:DWORD dst_unused:UNUSED_PAD src0_sel:DWORD src1_sel:WORD_0\n\t"
      "v_mad_u32_u16 v164, v164, s58, v250 op_sel:[1,0,0,0]\n\t"
      "s_waitcnt vmcnt(61)\n\t"
      "global_load_dword v164, v164, s[42:43] sc1\n\t"
      "v_xor_b32_sdwa v197, v246, v165 dst_sel:DWORD dst_unused:UNUSED_PAD src0_sel:DWORD src1_sel:WORD_0\n\t"
      "v_mad_u32_u16 v165, v165, s58, v250 op_sel:[1,0,0,0]\n\t"
      "s_waitcnt vmcnt(61)\n\t"
      "global_load_dword v165, v165, s[42:43] sc1\n\t"
      "s_cmp_gt_u32 s54, 40\n\t"
      "s_cselect_b64 vcc, -1, 0\n\t"
      "s_cmp_gt_u32 s54, 32\n\t"
      "s_cbranch_scc1 .LverB%=\n\t"
      "s_waitcnt vmcnt(57)\n\t"
      "v_smfmac_f32_16x16x64_bf16 v[38:41], a[32:35], v[70:77], v10\n\t"
      "ds_read_b128 v[102:105], v238 offset:512\n\t"
      "v_smfmac_f32_16x16x64_bf16 v[38:41], a[48:51], v[78:85], v12\n\t"
      "ds_read_b32 v214, v166 offset:0\n\t"
      "v_smfmac_f32_16x16x64_bf16 v[42:45], a[36:39], v[70:77], v10 abid:1\n\t"
      "ds_read_b128 v[106:109], v239 offset:512\n\t"
      "s_add_u32 s40, s40, 4096\n\t"
      "s_addc_u32 s41, s41, 0\n\t"
      "global_load_dwordx4 a[140:143], v245, s[40:41] sc1\n\t"
      "v_smfmac_f32_16x16x64_bf16 v[42:45], a[52:55], v[78:85], v12 abid:1\n\t"
      "ds_read_b32 v215, v167 offset:0\n\t"
      "v_smfmac_f32_16x16x64_bf16 v[46:49], a[40:43], v[70:77], v11\n\t"
      "ds_read_b128 v[110:113], v240 offset:512\n\t"
      "v_smfmac_f32_16x16x64_bf16 v[46:49], a[56:59], v[78:85], v13\n\t"
      "ds_read_b32 v216, v168 offset:0\n\t"
      "global_load_dwordx4 a[144:147], v245, s[40:41] offset:1024 sc1\n\t"
      "v_smfmac_f32_16x16x64_bf16 v[50:53], a[44:47], v[70:77], v11 abid:1\n\t"
      "ds_read_b128 v[114:117], v241 offset:512\n\t"
      "v_smfmac_f32_16x16x64_bf16 v[50:53], a[60:63], v[78:85], v13 abid:1\n\t"
      "s_mov_b64 exec, s[68:69]\n\t"
      "ds_read_b32 v217, v169 offset:0\n\t"
      "s_mov_b64 exec, -1\n\t"
      "v_smfmac_f32_16x16x64_bf16 v[54:57], a[32:35], v[86:93], v10\n\t"
      "ds_read_b128 v[70:73], v238 offset:2560\n\t"
      "global_load_dwordx4 a[148:151], v245, s[40:41] offset:2048 sc1\n\t"
      "v_smfmac_f32_16x16x64_bf16 v[54:57], a[48:51], v[94:101], v12\n\t"
      "ds_read_b32 v218, v170 offset:0\n\t"
      "v_smfmac_f32_16x16x64_bf16 v[58:61], a[36:39], v[86:93], v10 abid:1\n\t"
      "ds_read_b128 v[74:77], v239 offset:2560\n\t"
      "v_smfmac_f32_16x16x64_bf16 v[58:61], a[52:55], v[94:101], v12 abid:1\n\t"
      "ds_read_b32 v219, v171 offset:0\n\t"
      "global_load_dwordx4 a[152:155], v245, s[40:41] offset:3072 sc1\n\t"
      "v_smfmac_f32_16x16x64_bf16 v[62:65], a[40:43], v[86:93], v11\n\t"
      "ds_read_b128 v[78:81], v240 offset:2560\n\t"
      "v_smfmac_f32_16x16x64_bf16 v[62:65], a[56:59], v[94:101], v13\n\t"
      "ds_read_b32 v220, v172 offset:0\n\t"
      "v_smfmac_f32_16x16x64_bf16 v[66:69], a[44:47], v[86:93], v11 abid:1\n\t"
      "s_waitcnt lgkmcnt(8)\n\t"
      "ds_read_b128 v[82:85], v241 offset:2560\n\t"
      "s_add_u32 s40, s40, 4096\n\t"
      "s_addc_u32 s41, s41, 0\n\t"
      "global_load_dwordx4 a[156:159], v245, s[40:41] sc1\n\t"
      "v_smfmac_f32_16x16x64_bf16 v[66:69], a[60:63], v[94:101], v13 abid:1\n\t"
      "s_mov_b64 exec, s[68:69]\n\t"
      "ds_read_b32 v221, v173 offset:0\n\t"
      "s_mov_b64 exec, -1\n\t"
      "s_waitcnt vmcnt(52)\n\t"
      "s_waitcnt lgkmcnt(9)\n\t"
      "v_smfmac_f32_16x16x64_bf16 v[38:41], a[64:67], v[102:109], v14\n\t"
      "ds_read_b128 v[86:89], v238 offset:768\n\t"
      "v_smfmac_f32_16x16x64_bf16 v[38:41], a[80:83], v[110:117], v16\n\t"
      "ds_read_b32 v222, v174 offset:0\n\t"
      "global_load_dwordx2 v[26:27], v252, s[40:41] offset:1024 sc1\n\t"
      "global_load_dwordx2 v[28:29], v252, s[40:41] offset:1536 sc1\n\t"
      "v_smfmac_f32_16x16x64_bf16 v[42:45], a[68:71], v[102:109], v14 abid:1\n\t"
      "ds_read_b128 v[90:93], v239 offset:768\n\t"
      "v_smfmac_f32_16x16x64_bf16 v[42:45], a[84:87], v[110:117], v16 abid:1\n\t"
      "ds_read_b32 v223, v175 offset:0\n\t"
      "v_smfmac_f32_16x16x64_bf16 v[46:49], a[72:75], v[102:109], v15\n\t"
      "ds_read_b128 v[94:97], v240 offset:768\n\t"
      "global_load_dwordx4 a[160:163], v245, s[40:41] offset:2048 sc1\n\t"
      "v_smfmac_f32_16x16x64_bf16 v[46:49], a[88:91], v[110:117], v17\n\t"
      "s_waitcnt lgkmcnt(8)\n\t"
      "ds_read_b32 v224, v176 offset:0\n\t"
      "v_smfmac_f32_16x16x64_bf16 v[50:53], a[76:79], v[102:109], v15 abid:1\n\t"
      "ds_read_b128 v[98:101], v241 offset:768\n\t"
      "v_smfmac_f32_16x16x64_bf16 v[50:53], a[92:95], v[110:117], v17 abid:1\n\t"
      "s_mov_b64 exec, s[68:69]\n\t"
      "ds_read_b32 v225, v177 offset:0\n\t"
      "s_mov_b64 exec, -1\n\t"
      "global_load_dwordx4 a[164:167], v245, s[40:41] offset:3072 sc1\n\t"
      "s_waitcnt lgkmcnt(9)\n\t"
      "v_smfmac_f32_16x16x64_bf16 v[54:57], a[64:67], v[70:77], v14\n\t"
      "ds_read_b128 v[102:105], v238 offset:2816\n\t"
      "v_smfmac_f32_16x16x64_bf16 v[54:57], a[80:83], v[78:85], v16\n\t"
      "ds_read_b32 v226, v178 offset:0\n\t"
      "v_smfmac_f32_16x16x64_bf16 v[58:61], a[68:71], v[70:77], v14 abid:1\n\t"
      "ds_read_b128 v[106:109], v239 offset:2816\n\t"
      "s_add_u32 s40, s40, 4096\n\t"
      "s_addc_u32 s41, s41, 0\n\t"
      "global_load_dwordx4 a[168:171], v245, s[40:41] sc1\n\t"
      "v_smfmac_f32_16x16x64_bf16 v[58:61], a[84:87], v[78:85], v16 abid:1\n\t"
      "ds_read_b32 v227, v179 offset:0\n\t"
      "v_smfmac_f32_16x16x64_bf16 v[62:65], a[72:75], v[70:77], v15\n\t"
      "ds_read_b128 v[110:113], v240 offset:2816\n\t"
      "v_smfmac_f32_16x16x64_bf16 v[62:65], a[88:91], v[78:85], v17\n\t"
      "s_waitcnt lgkmcnt(8)\n\t"
      "ds_read_b32 v228, v180 offset:0\n\t"
      "global_load_dwordx4 a[172:175], v245, s[40:41] offset:1024 sc1\n\t"
      "v_smfmac_f32_16x16x64_bf16 v[66:69], a[76:79], v[70:77], v15 abid:1\n\t"
      "ds_read_b128 v[114:117], v241 offset:2816\n\t"
      "v_smfmac_f32_16x16x64_bf16 v[66:69], a[92:95], v[78:85], v17 abid:1\n\t"
      "s_mov_b64 exec, s[68:69]\n\t"
      "ds_read_b32 v229, v181 offset:0\n\t"
      "s_mov_b64 exec, -1\n\t"
      "s_waitcnt vmcnt(48)\n\t"
      "s_waitcnt lgkmcnt(9)\n\t"
      "v_smfmac_f32_16x16x64_bf16 v[38:41], a[96:99], v[86:93], v18\n\t"
      "ds_read_b128 v[70:73], v238 offset:1024\n\t"
      "global_load_dwordx4 a[176:179], v245, s[40:41] offset:2048 sc1\n\t"
      "v_smfmac_f32_16x16x64_bf16 v[38:41], a[112:115], v[94:101], v20\n\t"
      "ds_read_b32 v118, v182 offset:0\n\t"
      "v_smfmac_f32_16x16x64_bf16 v[42:45], a[100:103], v[86:93], v18 abid:1\n\t"
      "ds_read_b128 v[74:77], v239 offset:1024\n\t"
      "v_smfmac_f32_16x16x64_bf16 v[42:45], a[116:119], v[94:101], v20 abid:1\n\t"
      "ds_read_b32 v119, v183 offset:0\n\t"
      "global_load_dwordx4 a[180:183], v245, s[40:41] offset:3072 sc1\n\t"
      "v_smfmac_f32_16x16x64_bf16 v[46:49], a[104:107], v[86:93], v19\n\t"
      "ds_read_b128 v[78:81], v240 offset:1024\n\t"
      "v_smfmac_f32_16x16x64_bf16 v[46:49], a[120:123], v[94:101], v21\n\t"
      "s_waitcnt lgkmcnt(8)\n\t"
      "ds_read_b32 v120, v184 offset:0\n\t"
      "v_smfmac_f32_16x16x64_bf16 v[50:53], a[108:111], v[86:93], v19 abid:1\n\t"
      "ds_read_b128 v[82:85], v241 offset:1024\n\t"
      "s_add_u32 s40, s40, 4096\n\t"
      "s_addc_u32 s41, s41, 0\n\t"
      "global_load_dwordx4 a[184:187], v245, s[40:41] sc1\n\t"
      "v_smfmac_f32_16x16x64_bf16 v[50:53], a[124:127], v[94:101], v21 abid:1\n\t"
      "ds_read_b32 v121, v185 offset:0\n\t"
      "s_waitcnt vmcnt(14)\n\t"
      "s_waitcnt lgkmcnt(9)\n\t"
      "v_mfma_f32_16x16x32_bf16 v[38:41], v[134:137], v[214:217], v[38:41]\n\t"
      "ds_read_b128 v[86:89], v238 offset:3072\n\t"
      "v_mfma_f32_16x16x32_bf16 v[42:45], v[138:141], v[218:221], v[42:45]\n\t"
      "ds_read_b32 v122, v186 offset:0\n\t"
      "global_load_dwordx4 a[188:191], v245, s[40:41] offset:1024 sc1\n\t"
      "v_mfma_f32_16x16x32_bf16 v[46:49], v[142:145], v[222:225], v[46:49]\n\t"
      "ds_read_b128 v[90:93], v239 offset:3072\n\t"
      "s_waitcnt lgkmcnt(11)\n\t"
      "v_mfma_f32_16x16x32_bf16 v[50:53], v[146:149], v[226:229], v[50:53]\n\t"
      "ds_read_b32 v123, v187 offset:0\n\t"
      "s_waitcnt lgkmcnt(4)\n\t"
      "v_mfma_f32_16x16x32_bf16 v[38:41], v[150:153], v[118:121], v[38:41]\n\t"
      "ds_read_b128 v[94:97], v240 offset:3072\n\t"
      "global_load_dwordx2 v[30:31], v252, s[40:41] offset:2048 sc1\n\t"
      "global_load_dwordx2 v[32:33], v252, s[40:41] offset:2560 sc1\n\t"
      "ds_read_b32 v124, v188 offset:0\n\t"
      "ds_read_b32 v125, v189 offset:0\n\t"
      "s_waitcnt lgkmcnt(0)\n\t"
      "v_mfma_f32_16x16x32_bf16 v[42:45], v[154:157], v[122:125], v[42:45]\n\t"
      "ds_read_b32 v126, v190 offset:0\n\t"
      "ds_read_b32 v127, v191 offset:0\n\t"
      "ds_read_b32 v128, v192 offset:0\n\t"
      "ds_read_b32 v129, v193 offset:0\n\t"
      "s_waitcnt lgkmcnt(0)\n\t"
      "v_mfma_f32_16x16x32_bf16 v[46:49], v[158:161], v[126:129], v[46:49]\n\t"
      "ds_read_b128 v[98:101], v241 offset:3072\n\t"
      "ds_read_b32 v130, v194 offset:0\n\t"
      "ds_read_b32 v131, v195 offset:0\n\t"
      "ds_read_b32 v132, v196 offset:0\n\t"
      "ds_read_b32 v133, v197 offset:0\n\t"
      "s_waitcnt lgkmcnt(0)\n\t"
      "v_mfma_f32_16x16x32_bf16 v[50:53], v[162:165], v[130:133], v[50:53]\n\t"
      "global_load_dwordx4 a[192:195], v245, s[40:41] offset:3072 sc1\n\t"
      "v_smfmac_f32_16x16x64_bf16 v[54:57], a[96:99], v[102:109], v18\n\t"
      "ds_read_b32 v214, v166 offset:2048\n\t"
      "v_smfmac_f32_16x16x64_bf16 v[54:57], a[112:115], v[110:117], v20\n\t"
      "ds_read_b32 v215, v167 offset:2048\n\t"
      "v_smfmac_f32_16x16x64_bf16 v[58:61], a[100:103], v[102:109], v18 abid:1\n\t"
      "ds_read_b32 v216, v168 offset:2048\n\t"
      "s_add_u32 s40, s40, 4096\n\t"
      "s_addc_u32 s41, s41, 0\n\t"
      "global_load_dwordx4 a[196:199], v245, s[40:41] sc1\n\t"
      "v_smfmac_f32_16x16x64_bf16 v[58:61], a[116:119], v[110:117], v20 abid:1\n\t"
      "s_mov_b64 exec, s[68:69]\n\t"
      "ds_read_b32 v217, v169 offset:2048\n\t"
      "s_mov_b64 exec, -1\n\t"
      "v_smfmac_f32_16x16x64_bf16 v[62:65], a[104:107], v[102:109], v19\n\t"
      "ds_read_b32 v218, v170 offset:2048\n\t"
      "v_smfmac_f32_16x16x64_bf16 v[62:65], a[120:123], v[110:117], v21\n\t"
      "ds_read_b32 v219, v171 offset:2048\n\t"
      "global_load_dwordx4 a[200:203], v245, s[40:41] offset:1024 sc1\n\t"
      "v_smfmac_f32_16x16x64_bf16 v[66:69], a[108:111], v[102:109], v19 abid:1\n\t"
      "ds_read_b32 v220, v172 offset:2048\n\t"
      "v_smfmac_f32_16x16x64_bf16 v[66:69], a[124:127], v[110:117], v21 abid:1\n\t"
      "s_mov_b64 exec, s[68:69]\n\t"
      "ds_read_b32 v221, v173 offset:2048\n\t"
      "s_mov_b64 exec, -1\n\t"
      "s_waitcnt vmcnt(15)\n\t"
      "v_smfmac_f32_16x16x64_bf16 v[38:41], a[128:131], v[70:77], v22\n\t"
      "ds_read_b128 v[102:105], v238 offset:1280\n\t"
      "global_load_dwordx4 a[204:207], v245, s[40:41] offset:2048 sc1\n\t"
      "v_smfmac_f32_16x16x64_bf16 v[38:41], a[144:147], v[78:85], v24\n\t"
      "ds_read_b32 v222, v174 offset:2048\n\t"
      "v_smfmac_f32_16x16x64_bf16 v[42:45], a[132:135], v[70:77], v22 abid:1\n\t"
      "ds_read_b128 v[106:109], v239 offset:1280\n\t"
      "v_smfmac_f32_16x16x64_bf16 v[42:45], a[148:151], v[78:85], v24 abid:1\n\t"
      "ds_read_b32 v223, v175 offset:2048\n\t"
      "global_load_dwordx4 a[208:211], v245, s[40:41] offset:3072 sc1\n\t"
      "v_smfmac_f32_16x16x64_bf16 v[46:49], a[136:139], v[70:77], v23\n\t"
      "ds_read_b128 v[110:113], v240 offset:1280\n\t"
      "v_smfmac_f32_16x16x64_bf16 v[46:49], a[152:155], v[78:85], v25\n\t"
      "ds_read_b32 v224, v176 offset:2048\n\t"
      "v_smfmac_f32_16x16x64_bf16 v[50:53], a[140:143], v[70:77], v23 abid:1\n\t"
      "s_waitcnt lgkmcnt(8)\n\t"
      "ds_read_b128 v[114:117], v241 offset:1280\n\t"
      "s_add_u32 s40, s40, 4096\n\t"
      "s_addc_u32 s41, s41, 0\n\t"
      "global_load_dwordx4 a[212:215], v245, s[40:41] sc1\n\t"
      "v_smfmac_f32_16x16x64_bf16 v[50:53], a[156:159], v[78:85], v25 abid:1\n\t"
      "s_mov_b64 exec, s[68:69]\n\t"
      "ds_read_b32 v225, v177 offset:2048\n\t"
      "s_mov_b64 exec, -1\n\t"
      "v_smfmac_f32_16x16x64_bf16 v[54:57], a[128:131], v[86:93], v22\n\t"
      "ds_read_b128 v[70:73], v238 offset:3328\n\t"
      "v_smfmac_f32_16x16x64_bf16 v[54:57], a[144:147], v[94:101], v24\n\t"
      "ds_read_b32 v226, v178 offset:2048\n\t"
      "global_load_dwordx4 a[216:219], v245, s[40:41] offset:1024 sc1\n\t"
      "v_smfmac_f32_16x16x64_bf16 v[58:61], a[132:135], v[86:93], v22 abid:1\n\t"
      "ds_read_b128 v[74:77], v239 offset:3328\n\t"
      "v_smfmac_f32_16x16x64_bf16 v[58:61], a[148:151], v[94:101], v24 abid:1\n\t"
      "ds_read_b32 v227, v179 offset:2048\n\t"
      "v_smfmac_f32_16x16x64_bf16 v[62:65], a[136:139], v[86:93], v23\n\t"
      "s_waitcnt lgkmcnt(8)\n\t"
      "ds_read_b128 v[78:81], v240 offset:3328\n\t"
      "global_load_dwordx4 a[220:223], v245, s[40:41] offset:2048 sc1\n\t"
      "v_smfmac_f32_16x16x64_bf16 v[62:65], a[152:155], v[94:101], v25\n\t"
      "ds_read_b32 v228, v180 offset:2048\n\t"
      "v_smfmac_f32_16x16x64_bf16 v[66:69], a[140:143], v[86:93], v23 abid:1\n\t"
      "ds_read_b128 v[82:85], v241 offset:3328\n\t"
      "v_smfmac_f32_16x16x64_bf16 v[66:69], a[156:159], v[94:101], v25 abid:1\n\t"
      "s_mov_b64 exec, s[68:69]\n\t"
      "ds_read_b32 v229, v181 offset:2048\n\t"
      "s_mov_b64 exec, -1\n\t"
      "global_load_dwordx2 v[34:35], v252, s[40:41] offset:3072 sc1\n\t"
      "global_load_dwordx2 v[36:37], v252, s[40:41] offset:3584 sc1\n\t"
      "s_waitcnt vmcnt(12)\n\t"
      "s_waitcnt lgkmcnt(9)\n\t"
      "v_smfmac_f32_16x16x64_bf16 v[38:41], a[160:163], v[102:109], v26\n\t"
      "ds_read_b128 v[86:89], v238 offset:1536\n\t"
      "v_smfmac_f32_16x16x64_bf16 v[38:41], a[176:179], v[110:117], v28\n\t"
      "ds_read_b32 v118, v182 offset:2048\n\t"
      "v_smfmac_f32_16x16x64_bf16 v[42:45], a[164:167], v[102:109], v26 abid:1\n\t"
      "ds_read_b128 v[90:93], v239 offset:1536\n\t"
      "s_add_u32 s40, s40, 4096\n\t"
      "s_addc_u32 s41, s41, 0\n\t"
      "global_load_dwordx4 a[224:227], v245, s[40:41] sc1\n\t"
      "v_smfmac_f32_16x16x64_bf16 v[42:45], a[180:183], v[110:117], v28 abid:1\n\t"
      "ds_read_b32 v119, v183 offset:2048\n\t"
      "v_smfmac_f32_16x16x64_bf16 v[46:49], a[168:171], v[102:109], v27\n\t"
      "ds_read_b128 v[94:97], v240 offset:1536\n\t"
      "v_smfmac_f32_16x16x64_bf16 v[46:49], a[184:187], v[110:117], v29\n\t"
      "s_waitcnt lgkmcnt(8)\n\t"
      "ds_read_b32 v120, v184 offset:2048\n\t"
      "global_load_dwordx4 a[228:231], v245, s[40:41] offset:1024 sc1\n\t"
      "v_smfmac_f32_16x16x64_bf16 v[50:53], a[172:175], v[102:109], v27 abid:1\n\t"
      "ds_read_b128 v[98:101], v241 offset:1536\n\t"
      "v_smfmac_f32_16x16x64_bf16 v[50:53], a[188:191], v[110:117], v29 abid:1\n\t"
      "ds_read_b32 v121, v185 offset:2048\n\t"
      "s_waitcnt lgkmcnt(9)\n\t"
      "v_mfma_f32_16x16x32_bf16 v[54:57], v[134:137], v[214:217], v[54:57]\n\t"
      "ds_read_b128 v[102:105], v238 offset:3584\n\t"
      "global_load_dwordx4 a[232:235], v245, s[40:41] offset:2048 sc1\n\t"
      "v_mfma_f32_16x16x32_bf16 v[58:61], v[138:141], v[218:221], v[58:61]\n\t"
      "ds_read_b32 v122, v186 offset:2048\n\t"
      "v_mfma_f32_16x16x32_bf16 v[62:65], v[142:145], v[222:225], v[62:65]\n\t"
      "ds_read_b128 v[106:109], v239 offset:3584\n\t"
      "s_waitcnt lgkmcnt(11)\n\t"
      "v_mfma_f32_16x16x32_bf16 v[66:69], v[146:149], v[226:229], v[66:69]\n\t"
      "ds_read_b32 v123, v187 offset:2048\n\t"
      "global_load_dwordx4 a[236:239], v245, s[40:41] offset:3072 sc1\n\t"
      "s_waitcnt lgkmcnt(4)\n\t"
      "v_mfma_f32_16x16x32_bf16 v[54:57], v[150:153], v[118:121], v[54:57]\n\t"
      "ds_read_b128 v[110:113], v240 offset:3584\n\t"
      "ds_read_b32 v124, v188 offset:2048\n\t"
      "ds_read_b32 v125, v189 offset:2048\n\t"
      "s_waitcnt lgkmcnt(0)\n\t"
      "v_mfma_f32_16x16x32_bf16 v[58:61], v[154:157], v[122:125], v[58:61]\n\t"
      "ds_read_b32 v126, v190 offset:2048\n\t"
      "ds_read_b32 v127, v191 offset:2048\n\t"
      "ds_read_b32 v128, v192 offset:2048\n\t"
      "ds_read_b32 v129, v193 offset:2048\n\t"
      "s_waitcnt lgkmcnt(0)\n\t"
      "v_mfma_f32_16x16x32_bf16 v[62:65], v[158:161], v[126:129], v[62:65]\n\t"
      "ds_read_b128 v[114:117], v241 offset:3584\n\t"
      "s_add_u32 s40, s40, 4096\n\t"
      "s_addc_u32 s41, s41, 0\n\t"
      "global_load_dwordx4 a[240:243], v245, s[40:41] sc1\n\t"
      "ds_read_b32 v130, v194 offset:2048\n\t"
      "ds_read_b32 v131, v195 offset:2048\n\t"
      "ds_read_b32 v132, v196 offset:2048\n\t"
      "ds_read_b32 v133, v197 offset:2048\n\t"
      "s_waitcnt lgkmcnt(0)\n\t"
      "v_mfma_f32_16x16x32_bf16 v[66:69], v[162:165], v[130:133], v[66:69]\n\t"
      "v_xor_b32 v166, s57, v166\n\t"
      "v_xor_b32 v167, s57, v167\n\t"
      "v_xor_b32 v168, s57, v168\n\t"
      "v_xor_b32 v169, s57, v169\n\t"
      "v_xor_b32 v170, s57, v170\n\t"
      "v_xor_b32 v171, s57, v171\n\t"
      "v_xor_b32 v172, s57, v172\n\t"
      "v_xor_b32 v173, s57, v173\n\t"
      "v_xor_b32 v174, s57, v174\n\t"
      "v_xor_b32 v175, s57, v175\n\t"
      "v_xor_b32 v176, s57, v176\n\t"
      "v_xor_b32 v177, s57, v177\n\t"
      "v_xor_b32 v178, s57, v178\n\t"
      "v_xor_b32 v179, s57, v179\n\t"
      "v_xor_b32 v180, s57, v180\n\t"
      "v_xor_b32 v181, s57, v181\n\t"
      "v_smfmac_f32_16x16x64_bf16 v[54:57], a[160:163], v[70:77], v26\n\t"
      "ds_read_b32 v214, v166 offset:0\n\t"
      "v_xor_b32 v182, s57, v182\n\t"
      "v_smfmac_f32_16x16x64_bf16 v[54:57], a[176:179], v[78:85], v28\n\t"
      "ds_read_b32 v215, v167 offset:0\n\t"
      "v_xor_b32 v183, s57, v183\n\t"
      "global_load_dwordx4 a[244:247], v245, s[40:41] offset:1024 sc1\n\t"
      "v_smfmac_f32_16x16x64_bf16 v[58:61], a[164:167], v[70:77], v26 abid:1\n\t"
      "ds_read_b32 v216, v168 offset:0\n\t"
      "v_xor_b32 v184, s57, v184\n\t"
      "v_smfmac_f32_16x16x64_bf16 v[58:61], a[180:183], v[78:85], v28 abid:1\n\t"
      "s_mov_b64 exec, s[68:69]\n\t"
      "ds_read_b32 v217, v169 offset:0\n\t"
      "s_mov_b64 exec, -1\n\t"
      "v_xor_b32 v185, s57, v185\n\t"
      "v_smfmac_f32_16x16x64_bf16 v[62:65], a[168:171], v[70:77], v27\n\t"
      "ds_read_b32 v218, v170 offset:0\n\t"
      "v_xor_b32 v186, s57, v186\n\t"
      "global_load_dwordx4 a[248:251], v245, s[40:41] offset:2048 sc1\n\t"
      "v_smfmac_f32_16x16x64_bf16 v[62:65], a[184:187], v[78:85], v29\n\t"
      "ds_read_b32 v219, v171 offset:0\n\t"
      "v_xor_b32 v187, s57, v187\n\t"
      "v_smfmac_f32_16x16x64_bf16 v[66:69], a[172:175], v[70:77], v27 abid:1\n\t"
      "ds_read_b32 v220, v172 offset:0\n\t"
      "v_xor_b32 v188, s57, v188\n\t"
      "v_smfmac_f32_16x16x64_bf16 v[66:69], a[188:191], v[78:85], v29 abid:1\n\t"
      "s_mov_b64 exec, s[68:69]\n\t"
      "ds_read_b32 v221, v173 offset:0\n\t"
      "s_mov_b64 exec, -1\n\t"
      "v_xor_b32 v189, s57, v189\n\t"
      "global_load_dwordx4 a[252:255], v245, s[40:41] offset:3072 sc1\n\t"
      "s_waitcnt vmcnt(10)\n\t"
      "v_smfmac_f32_16x16x64_bf16 v[38:41], a[192:195], v[86:93], v30\n\t"
      "ds_read_b128 v[70:73], v238 offset:1792\n\t"
      "v_xor_b32 v190, s57, v190\n\t"
      "v_smfmac_f32_16x16x64_bf16 v[38:41], a[208:211], v[94:101], v32\n\t"
      "ds_read_b32 v222, v174 offset:0\n\t"
      "v_xor_b32 v191, s57, v191\n\t"
      "v_smfmac_f32_16x16x64_bf16 v[42:45], a[196:199], v[86:93], v30 abid:1\n\t"
      "ds_read_b128 v[74:77], v239 offset:1792\n\t"
      "v_xor_b32 v192, s57, v192\n\t"
      "v_smfmac_f32_16x16x64_bf16 v[42:45], a[212:215], v[94:101], v32 abid:1\n\t"
      "ds_read_b32 v223, v175 offset:0\n\t"
      "v_xor_b32 v193, s57, v193\n\t"
      "v_smfmac_f32_16x16x64_bf16 v[46:49], a[200:203], v[86:93], v31\n\t"
      "ds_read_b128 v[78:81], v240 offset:1792\n\t"
      "v_xor_b32 v194, s57, v194\n\t"
      "v_smfmac_f32_16x16x64_bf16 v[46:49], a[216:219], v[94:101], v33\n\t"
      "ds_read_b32 v224, v176 offset:0\n\t"
      "v_xor_b32 v195, s57, v195\n\t"
      "v_smfmac_f32_16x16x64_bf16 v[50:53], a[204:207], v[86:93], v31 abid:1\n\t"
      "s_waitcnt lgkmcnt(8)\n\t"
      "ds_read_b128 v[82:85], v241 offset:1792\n\t"
      "v_xor_b32 v196, s57, v196\n\t"
      "v_smfmac_f32_16x16x64_bf16 v[50:53], a[220:223], v[94:101], v33 abid:1\n\t"
      "s_mov_b64 exec, s[68:69]\n\t"
      "ds_read_b32 v225, v177 offset:0\n\t"
      "s_mov_b64 exec, -1\n\t"
      "v_xor_b32 v197, s57, v197\n\t"
      "v_smfmac_f32_16x16x64_bf16 v[54:57], a[192:195], v[102:109], v30\n\t"
      "ds_read_b128 v[86:89], v238 offset:3840\n\t"
      "v_smfmac_f32_16x16x64_bf16 v[54:57], a[208:211], v[110:117], v32\n\t"
      "ds_read_b32 v226, v178 offset:0\n\t"
      "v_smfmac_f32_16x16x64_bf16 v[58:61], a[196:199], v[102:109], v30 abid:1\n\t"
      "ds_read_b128 v[90:93], v239 offset:3840\n\t"
      "v_smfmac_f32_16x16x64_bf16 v[58:61], a[212:215], v[110:117], v32 abid:1\n\t"
      "ds_read_b32 v227, v179 offset:0\n\t"
      "v_smfmac_f32_16x16x64_bf16 v[62:65], a[200:203], v[102:109], v31\n\t"
      "s_waitcnt lgkmcnt(8)\n\t"
      "ds_read_b128 v[94:97], v240 offset:3840\n\t"
      "v_smfmac_f32_16x16x64_bf16 v[62:65], a[216:219], v[110:117], v33\n\t"
      "ds_read_b32 v228, v180 offset:0\n\t"
      "v_smfmac_f32_16x16x64_bf16 v[66:69], a[204:207], v[102:109], v31 abid:1\n\t"
      "ds_read_b128 v[98:101], v241 offset:3840\n\t"
      "v_smfmac_f32_16x16x64_bf16 v[66:69], a[220:223], v[110:117], v33 abid:1\n\t"
      "s_mov_b64 exec, s[68:69]\n\t"
      "ds_read_b32 v229, v181 offset:0\n\t"
      "s_mov_b64 exec, -1\n\t"
      "s_waitcnt vmcnt(0)\n\t"
      "s_waitcnt lgkmcnt(9)\n\t"
      "v_smfmac_f32_16x16x64_bf16 v[38:41], a[224:227], v[70:77], v34\n\t"
      "v_smfmac_f32_16x16x64_bf16 v[38:41], a[240:243], v[78:85], v36\n\t"
      "v_smfmac_f32_16x16x64_bf16 v[42:45], a[228:231], v[70:77], v34 abid:1\n\t"
      "v_smfmac_f32_16x16x64_bf16 v[42:45], a[244:247], v[78:85], v36 abid:1\n\t"
      "v_smfmac_f32_16x16x64_bf16 v[46:49], a[232:235], v[70:77], v35\n\t"
      "v_smfmac_f32_16x16x64_bf16 v[46:49], a[248:251], v[78:85], v37\n\t"
      "v_smfmac_f32_16x16x64_bf16 v[50:53], a[236:239], v[70:77], v35 abid:1\n\t"
      "v_smfmac_f32_16x16x64_bf16 v[50:53], a[252:255], v[78:85], v37 abid:1\n\t"
      "v_and_b32 v249, 7, %0\n\t"
      "v_bfe_u32 v250, %0, 3, 1\n\t"
      "v_lshlrev_b32 v244, 4, v248\n\t"
      "v_lshl_add_u32 v244, v250, 6, v244\n\t"
      "v_lshl_add_u32 v244, v249, 11, v244\n\t"
      "v_add_u32 v252, 16384, v244\n\t"
      "v_and_b32 v249, 12, v247\n\t"
      "global_load_dword v242, v249, %8 offset:2048 sc1\n\t"
      "s_barrier\n\t"
      "v_xor_b32 v238, s57, v238\n\t"
      "v_xor_b32 v239, s57, v239\n\t"
      "v_xor_b32 v240, s57, v240\n\t"
      "v_xor_b32 v241, s57, v241\n\t"
      "s_waitcnt lgkmcnt(1)\n\t"
      "v_smfmac_f32_16x16x64_bf16 v[54:57], a[224:227], v[86:93], v34\n\t"
      "ds_read_b128 v[70:73], v238 offset:0\n\t"
      "v_cvt_pk_bf16_f32 v38, v38, v39\n\t"
      "s_add_u32 m0, s46, 0\n\t"
      "s_nop 0\n\t"
      "global_load_lds_dwordx4 %11, s[36:37]\n\t"
      "v_smfmac_f32_16x16x64_bf16 v[54:57], a[240:243], v[94:101], v36\n\t"
      "ds_read_b128 v[74:77], v239 offset:0\n\t"
      "v_cvt_pk_bf16_f32 v39, v40, v41\n\t"
      "global_load_lds_dwordx4 %11, s[36:37] offset:1024\n\t"
      "v_smfmac_f32_16x16x64_bf16 v[58:61], a[228:231], v[86:93], v34 abid:1\n\t"
      "ds_read_b128 v[78:81], v240 offset:0\n\t"
      "v_cvt_pk_bf16_f32 v40, v42, v43\n\t"
      "s_add_u32 m0, s47, 0\n\t"
      "s_nop 0\n\t"
      "global_load_lds_dwordx4 %12, s[36:37]\n\t"
      "v_smfmac_f32_16x16x64_bf16 v[58:61], a[244:247], v[94:101], v36 abid:1\n\t"
      "ds_read_b128 v[82:85], v241 offset:0\n\t"
      "v_cvt_pk_bf16_f32 v41, v44, v45\n\t"
      "global_load_lds_dwordx4 %12, s[36:37] offset:1024\n\t"
      "v_smfmac_f32_16x16x64_bf16 v[62:65], a[232:235], v[86:93], v35\n\t"
      "v_cvt_pk_bf16_f32 v46, v46, v47\n\t"
      "s_add_u32 m0, s48, 0\n\t"
      "s_nop 0\n\t"
      "global_load_lds_dwordx4 %13, s[36:37]\n\t"
      "v_smfmac_f32_16x16x64_bf16 v[62:65], a[248:251], v[94:101], v37\n\t"
      "v_cvt_pk_bf16_f32 v47, v48, v49\n\t"
      "global_load_lds_dwordx4 %13, s[36:37] offset:1024\n\t"
      "v_smfmac_f32_16x16x64_bf16 v[66:69], a[236:239], v[86:93], v35 abid:1\n\t"
      "v_cvt_pk_bf16_f32 v48, v50, v51\n\t"
      "s_add_u32 m0, s49, 0\n\t"
      "s_nop 0\n\t"
      "global_load_lds_dwordx4 %14, s[36:37]\n\t"
      "v_smfmac_f32_16x16x64_bf16 v[66:69], a[252:255], v[94:101], v37 abid:1\n\t"
      "v_cvt_pk_bf16_f32 v49, v52, v53\n\t"
      "global_load_lds_dwordx4 %14, s[36:37] offset:1024\n\t"
      "s_add_u32 s36, s36, 2097152\n\t"
      "s_addc_u32 s37, s37, 0\n\t"
      "s_nop 1\n\t"
      "v_mov_b32_dpp v42, v46 row_ror:8 row_mask:0xf bank_mask:0xf\n\t"
      "v_mov_b32_dpp v43, v47 row_ror:8 row_mask:0xf bank_mask:0xf\n\t"
      "v_mov_b32_dpp v44, v48 row_ror:8 row_mask:0xf bank_mask:0xf\n\t"
      "v_mov_b32_dpp v45, v49 row_ror:8 row_mask:0xf bank_mask:0xf\n\t"
      "v_mov_b32_dpp v46, v38 row_ror:8 row_mask:0xf bank_mask:0x3\n\t"
      "v_mov_b32_dpp v47, v39 row_ror:8 row_mask:0xf bank_mask:0x3\n\t"
      "v_mov_b32_dpp v48, v40 row_ror:8 row_mask:0xf bank_mask:0x3\n\t"
      "v_mov_b32_dpp v49, v41 row_ror:8 row_mask:0xf bank_mask:0x3\n\t"
      "v_mov_b32_dpp v38, v42 quad_perm:[0,1,2,3] row_mask:0xf bank_mask:0xc\n\t"
      "v_mov_b32_dpp v39, v43 quad_perm:[0,1,2,3] row_mask:0xf bank_mask:0xc\n\t"
      "v_mov_b32_dpp v40, v44 quad_perm:[0,1,2,3] row_mask:0xf bank_mask:0xc\n\t"
      "v_mov_b32_dpp v41, v45 quad_perm:[0,1,2,3] row_mask:0xf bank_mask:0xc\n\t"
      "global_store_dwordx4 v244, v[38:41], s[38:39] sc1\n\t"
      "global_store_dwordx4 v252, v[46:49], s[38:39] sc1\n\t"
      "s_add_u32 s38, s38, 2097152\n\t"
      "s_addc_u32 s39, s39, 0\n\t"
      "s_waitcnt lgkmcnt(0)\n\t"
      ".LloopA%=:\n\t"
      "s_waitcnt lgkmcnt(0)\n\t"
      "v_mfma_f32_16x16x32_bf16 v[38:41], v[134:137], v[214:217], 0\n\t"
      "ds_read_b128 v[86:89], v238 offset:256\n\t"
      "v_mfma_f32_16x16x32_bf16 v[42:45], v[138:141], v[218:221], 0\n\t"
      "ds_read_b32 v118, v182 offset:0\n\t"
      "v_mfma_f32_16x16x32_bf16 v[46:49], v[142:145], v[222:225], 0\n\t"
      "ds_read_b128 v[90:93], v239 offset:256\n\t"
      "v_mfma_f32_16x16x32_bf16 v[50:53], v[146:149], v[226:229], 0\n\t"
      "ds_read_b32 v119, v183 offset:0\n\t"
      "v_smfmac_f32_16x16x64_bf16 v[38:41], a[0:3], v[70:77], v6\n\t"
      "ds_read_b128 v[94:97], v240 offset:256\n\t"
      "v_smfmac_f32_16x16x64_bf16 v[38:41], a[16:19], v[78:85], v8\n\t"
      "ds_read_b32 v120, v184 offset:0\n\t"
      "v_smfmac_f32_16x16x64_bf16 v[42:45], a[4:7], v[70:77], v6 abid:1\n\t"
      "ds_read_b128 v[98:101], v241 offset:256\n\t"
      "v_smfmac_f32_16x16x64_bf16 v[42:45], a[20:23], v[78:85], v8 abid:1\n\t"
      "ds_read_b32 v121, v185 offset:0\n\t"
      "v_smfmac_f32_16x16x64_bf16 v[46:49], a[8:11], v[70:77], v7\n\t"
      "ds_read_b128 v[102:105], v238 offset:512\n\t"
      "v_smfmac_f32_16x16x64_bf16 v[46:49], a[24:27], v[78:85], v9\n\t"
      "ds_read_b32 v122, v186 offset:0\n\t"
      "v_smfmac_f32_16x16x64_bf16 v[50:53], a[12:15], v[70:77], v7 abid:1\n\t"
      "ds_read_b128 v[106:109], v239 offset:512\n\t"
      "v_smfmac_f32_16x16x64_bf16 v[50:53], a[28:31], v[78:85], v9 abid:1\n\t"
      "ds_read_b32 v123, v187 offset:0\n\t"
      "s_waitcnt lgkmcnt(5)\n\t"
      "v_smfmac_f32_16x16x64_bf16 v[38:41], a[32:35], v[86:93], v10\n\t"
      "ds_read_b128 v[110:113], v240 offset:512\n\t"
      "v_cvt_pk_bf16_f32 v54, v54, v55\n\t"
      "v_smfmac_f32_16x16x64_bf16 v[38:41], a[48:51], v[94:101], v12\n\t"
      "ds_read_b32 v124, v188 offset:0\n\t"
      "v_cvt_pk_bf16_f32 v55, v56, v57\n\t"
      "v_smfmac_f32_16x16x64_bf16 v[42:45], a[36:39], v[86:93], v10 abid:1\n\t"
      "ds_read_b128 v[114:117], v241 offset:512\n\t"
      "v_cvt_pk_bf16_f32 v56, v58, v59\n\t"
      "v_smfmac_f32_16x16x64_bf16 v[42:45], a[52:55], v[94:101], v12 abid:1\n\t"
      "ds_read_b32 v125, v189 offset:0\n\t"
      "v_cvt_pk_bf16_f32 v57, v60, v61\n\t"
      "v_smfmac_f32_16x16x64_bf16 v[46:49], a[40:43], v[86:93], v11\n\t"
      "ds_read_b128 v[70:73], v238 offset:768\n\t"
      "v_cvt_pk_bf16_f32 v62, v62, v63\n\t"
      "v_smfmac_f32_16x16x64_bf16 v[46:49], a[56:59], v[94:101], v13\n\t"
      "ds_read_b32 v126, v190 offset:0\n\t"
      "v_cvt_pk_bf16_f32 v63, v64, v65\n\t"
      "v_smfmac_f32_16x16x64_bf16 v[50:53], a[44:47], v[86:93], v11 abid:1\n\t"
      "ds_read_b128 v[74:77], v239 offset:768\n\t"
      "v_cvt_pk_bf16_f32 v64, v66, v67\n\t"
      "v_smfmac_f32_16x16x64_bf16 v[50:53], a[60:63], v[94:101], v13 abid:1\n\t"
      "ds_read_b32 v127, v191 offset:0\n\t"
      "v_cvt_pk_bf16_f32 v65, v68, v69\n\t"
      "s_waitcnt lgkmcnt(5)\n\t"
      "v_smfmac_f32_16x16x64_bf16 v[38:41], a[64:67], v[102:109], v14\n\t"
      "ds_read_b128 v[78:81], v240 offset:768\n\t"
      "v_mov_b32_dpp v58, v62 row_ror:8 row_mask:0xf bank_mask:0xf\n\t"
      "v_smfmac_f32_16x16x64_bf16 v[38:41], a[80:83], v[110:117], v16\n\t"
      "ds_read_b32 v128, v192 offset:0\n\t"
      "v_mov_b32_dpp v59, v63 row_ror:8 row_mask:0xf bank_mask:0xf\n\t"
      "v_smfmac_f32_16x16x64_bf16 v[42:45], a[68:71], v[102:109], v14 abid:1\n\t"
      "ds_read_b128 v[82:85], v241 offset:768\n\t"
      "v_mov_b32_dpp v60, v64 row_ror:8 row_mask:0xf bank_mask:0xf\n\t"
      "v_smfmac_f32_16x16x64_bf16 v[42:45], a[84:87], v[110:117], v16 abid:1\n\t"
      "ds_read_b32 v129, v193 offset:0\n\t"
      "v_mov_b32_dpp v61, v65 row_ror:8 row_mask:0xf bank_mask:0xf\n\t"
      "v_smfmac_f32_16x16x64_bf16 v[46:49], a[72:75], v[102:109], v15\n\t"
      "ds_read_b128 v[86:89], v238 offset:1024\n\t"
      "v_mov_b32_dpp v62, v54 row_ror:8 row_mask:0xf bank_mask:0x3\n\t"
      "v_smfmac_f32_16x16x64_bf16 v[46:49], a[88:91], v[110:117], v17\n\t"
      "ds_read_b32 v130, v194 offset:0\n\t"
      "v_mov_b32_dpp v63, v55 row_ror:8 row_mask:0xf bank_mask:0x3\n\t"
      "v_smfmac_f32_16x16x64_bf16 v[50:53], a[76:79], v[102:109], v15 abid:1\n\t"
      "ds_read_b128 v[90:93], v239 offset:1024\n\t"
      "v_mov_b32_dpp v64, v56 row_ror:8 row_mask:0xf bank_mask:0x3\n\t"
      "v_smfmac_f32_16x16x64_bf16 v[50:53], a[92:95], v[110:117], v17 abid:1\n\t"
      "ds_read_b32 v131, v195 offset:0\n\t"
      "v_mov_b32_dpp v65, v57 row_ror:8 row_mask:0xf bank_mask:0x3\n\t"
      "s_waitcnt vmcnt(12)\n\t"
      "s_barrier\n\t"
      "s_xor_b32 s71, s67, 6144\n\t"
      "s_waitcnt lgkmcnt(5)\n\t"
      "v_smfmac_f32_16x16x64_bf16 v[38:41], a[96:99], v[70:77], v18\n\t"
      "ds_read_b128 v[94:97], v240 offset:1024\n\t"
      "v_mov_b32_dpp v54, v58 quad_perm:[0,1,2,3] row_mask:0xf bank_mask:0xc\n\t"
      "s_add_u32 m0, s46, s71\n\t"
      "s_nop 0\n\t"
      "global_load_lds_dwordx4 %11, s[36:37]\n\t"
      "v_smfmac_f32_16x16x64_bf16 v[38:41], a[112:115], v[78:85], v20\n\t"
      "ds_read_b32 v132, v196 offset:0\n\t"
      "v_mov_b32_dpp v55, v59 quad_perm:[0,1,2,3] row_mask:0xf bank_mask:0xc\n\t"
      "v_smfmac_f32_16x16x64_bf16 v[42:45], a[100:103], v[70:77], v18 abid:1\n\t"
      "ds_read_b128 v[98:101], v241 offset:1024\n\t"
      "v_mov_b32_dpp v56, v60 quad_perm:[0,1,2,3] row_mask:0xf bank_mask:0xc\n\t"
      "v_smfmac_f32_16x16x64_bf16 v[42:45], a[116:119], v[78:85], v20 abid:1\n\t"
      "ds_read_b32 v133, v197 offset:0\n\t"
      "v_mov_b32_dpp v57, v61 quad_perm:[0,1,2,3] row_mask:0xf bank_mask:0xc\n\t"
      "global_load_lds_dwordx4 %11, s[36:37] offset:1024\n\t"
      "v_smfmac_f32_16x16x64_bf16 v[46:49], a[104:107], v[70:77], v19\n\t"
      "ds_read_b128 v[102:105], v238 offset:1280\n\t"
      "v_smfmac_f32_16x16x64_bf16 v[46:49], a[120:123], v[78:85], v21\n\t"
      "ds_read_b128 v[106:109], v239 offset:1280\n\t"
      "v_smfmac_f32_16x16x64_bf16 v[50:53], a[108:111], v[70:77], v19 abid:1\n\t"
      "ds_read_b128 v[110:113], v240 offset:1280\n\t"
      "s_add_u32 m0, s47, s71\n\t"
      "s_nop 0\n\t"
      "global_load_lds_dwordx4 %12, s[36:37]\n\t"
      "v_smfmac_f32_16x16x64_bf16 v[50:53], a[124:127], v[78:85], v21 abid:1\n\t"
      "ds_read_b128 v[114:117], v241 offset:1280\n\t"
      "s_waitcnt lgkmcnt(5)\n\t"
      "v_mfma_f32_16x16x32_bf16 v[38:41], v[150:153], v[118:121], v[38:41]\n\t"
      "ds_read_b128 v[70:73], v238 offset:1536\n\t"
      "v_mfma_f32_16x16x32_bf16 v[42:45], v[154:157], v[122:125], v[42:45]\n\t"
      "ds_read_b32 v214, v166 offset:2048\n\t"
      "global_load_lds_dwordx4 %12, s[36:37] offset:1024\n\t"
      "v_mfma_f32_16x16x32_bf16 v[46:49], v[158:161], v[126:129], v[46:49]\n\t"
      "ds_read_b128 v[74:77], v239 offset:1536\n\t"
      "s_waitcnt lgkmcnt(7)\n\t"
      "v_mfma_f32_16x16x32_bf16 v[50:53], v[162:165], v[130:133], v[50:53]\n\t"
      "ds_read_b32 v215, v167 offset:2048\n\t"
      "v_smfmac_f32_16x16x64_bf16 v[38:41], a[128:131], v[86:93], v22\n\t"
      "ds_read_b128 v[78:81], v240 offset:1536\n\t"
      "s_add_u32 m0, s48, s71\n\t"
      "s_nop 0\n\t"
      "global_load_lds_dwordx4 %13, s[36:37]\n\t"
      "v_smfmac_f32_16x16x64_bf16 v[38:41], a[144:147], v[94:101], v24\n\t"
      "ds_read_b32 v216, v168 offset:2048\n\t"
      "v_smfmac_f32_16x16x64_bf16 v[42:45], a[132:135], v[86:93], v22 abid:1\n\t"
      "ds_read_b128 v[82:85], v241 offset:1536\n\t"
      "v_smfmac_f32_16x16x64_bf16 v[42:45], a[148:151], v[94:101], v24 abid:1\n\t"
      "s_mov_b64 exec, s[68:69]\n\t"
      "ds_read_b32 v217, v169 offset:2048\n\t"
      "s_mov_b64 exec, -1\n\t"
      "global_load_lds_dwordx4 %13, s[36:37] offset:1024\n\t"
      "v_smfmac_f32_16x16x64_bf16 v[46:49], a[136:139], v[86:93], v23\n\t"
      "ds_read_b32 v218, v170 offset:2048\n\t"
      "v_smfmac_f32_16x16x64_bf16 v[46:49], a[152:155], v[94:101], v25\n\t"
      "ds_read_b32 v219, v171 offset:2048\n\t"
      "v_smfmac_f32_16x16x64_bf16 v[50:53], a[140:143], v[86:93], v23 abid:1\n\t"
      "s_waitcnt lgkmcnt(8)\n\t"
      "ds_read_b32 v220, v172 offset:2048\n\t"
      "s_add_u32 m0, s49, s71\n\t"
      "s_nop 0\n\t"
      "global_load_lds_dwordx4 %14, s[36:37]\n\t"
      "v_smfmac_f32_16x16x64_bf16 v[50:53], a[156:159], v[94:101], v25 abid:1\n\t"
      "s_mov_b64 exec, s[68:69]\n\t"
      "ds_read_b32 v221, v173 offset:2048\n\t"
      "s_mov_b64 exec, -1\n\t"
      "v_smfmac_f32_16x16x64_bf16 v[38:41], a[160:163], v[102:109], v26\n\t"
      "ds_read_b128 v[86:89], v238 offset:1792\n\t"
      "v_smfmac_f32_16x16x64_bf16 v[38:41], a[176:179], v[110:117], v28\n\t"
      "ds_read_b32 v222, v174 offset:2048\n\t"
      "global_load_lds_dwordx4 %14, s[36:37] offset:1024\n\t"
      "s_add_u32 s36, s36, s73\n\t"
      "s_addc_u32 s37, s37, 0\n\t"
      "v_smfmac_f32_16x16x64_bf16 v[42:45], a[164:167], v[102:109], v26 abid:1\n\t"
      "ds_read_b128 v[90:93], v239 offset:1792\n\t"
      "v_smfmac_f32_16x16x64_bf16 v[42:45], a[180:183], v[110:117], v28 abid:1\n\t"
      "ds_read_b32 v223, v175 offset:2048\n\t"
      "v_smfmac_f32_16x16x64_bf16 v[46:49], a[168:171], v[102:109], v27\n\t"
      "s_waitcnt lgkmcnt(8)\n\t"
      "ds_read_b128 v[94:97], v240 offset:1792\n\t"
      "v_smfmac_f32_16x16x64_bf16 v[46:49], a[184:187], v[110:117], v29\n\t"
      "ds_read_b32 v224, v176 offset:2048\n\t"
      "v_smfmac_f32_16x16x64_bf16 v[50:53], a[172:175], v[102:109], v27 abid:1\n\t"
      "ds_read_b128 v[98:101], v241 offset:1792\n\t"
      "v_smfmac_f32_16x16x64_bf16 v[50:53], a[188:191], v[110:117], v29 abid:1\n\t"
      "s_mov_b64 exec, s[68:69]\n\t"
      "ds_read_b32 v225, v177 offset:2048\n\t"
      "s_mov_b64 exec, -1\n\t"
      "v_smfmac_f32_16x16x64_bf16 v[38:41], a[192:195], v[70:77], v30\n\t"
      "ds_read_b128 v[102:105], v238 offset:2048\n\t"
      "v_smfmac_f32_16x16x64_bf16 v[38:41], a[208:211], v[78:85], v32\n\t"
      "ds_read_b32 v226, v178 offset:2048\n\t"
      "v_smfmac_f32_16x16x64_bf16 v[42:45], a[196:199], v[70:77], v30 abid:1\n\t"
      "s_waitcnt lgkmcnt(8)\n\t"
      "ds_read_b128 v[106:109], v239 offset:2048\n\t"
      "v_smfmac_f32_16x16x64_bf16 v[42:45], a[212:215], v[78:85], v32 abid:1\n\t"
      "ds_read_b32 v227, v179 offset:2048\n\t"
      "v_smfmac_f32_16x16x64_bf16 v[46:49], a[200:203], v[70:77], v31\n\t"
      "ds_read_b128 v[110:113], v240 offset:2048\n\t"
      "v_smfmac_f32_16x16x64_bf16 v[46:49], a[216:219], v[78:85], v33\n\t"
      "ds_read_b32 v228, v180 offset:2048\n\t"
      "v_smfmac_f32_16x16x64_bf16 v[50:53], a[204:207], v[70:77], v31 abid:1\n\t"
      "ds_read_b128 v[114:117], v241 offset:2048\n\t"
      "v_smfmac_f32_16x16x64_bf16 v[50:53], a[220:223], v[78:85], v33 abid:1\n\t"
      "s_mov_b64 exec, s[68:69]\n\t"
      "ds_read_b32 v229, v181 offset:2048\n\t"
      "s_mov_b64 exec, -1\n\t"
      "s_waitcnt lgkmcnt(9)\n\t"
      "v_smfmac_f32_16x16x64_bf16 v[38:41], a[224:227], v[86:93], v34\n\t"
      "ds_read_b128 v[70:73], v238 offset:2304\n\t"
      "global_store_dwordx4 v244, v[54:57], s[38:39] sc1\n\t"
      "v_smfmac_f32_16x16x64_bf16 v[38:41], a[240:243], v[94:101], v36\n\t"
      "ds_read_b128 v[74:77], v239 offset:2304\n\t"
      "v_smfmac_f32_16x16x64_bf16 v[42:45], a[228:231], v[86:93], v34 abid:1\n\t"
      "ds_read_b128 v[78:81], v240 offset:2304\n\t"
      "global_store_dwordx4 v252, v[62:65], s[38:39] sc1\n\t"
      "v_smfmac_f32_16x16x64_bf16 v[42:45], a[244:247], v[94:101], v36 abid:1\n\t"
      "ds_read_b128 v[82:85], v241 offset:2304\n\t"
      "v_smfmac_f32_16x16x64_bf16 v[46:49], a[232:235], v[86:93], v35\n\t"
      "s_add_u32 s38, s38, 2097152\n\t"
      "s_addc_u32 s39, s39, 0\n\t"
      "v_smfmac_f32_16x16x64_bf16 v[46:49], a[248:251], v[94:101], v37\n\t"
      "v_smfmac_f32_16x16x64_bf16 v[50:53], a[236:239], v[86:93], v35 abid:1\n\t"
      "v_smfmac_f32_16x16x64_bf16 v[50:53], a[252:255], v[94:101], v37 abid:1\n\t"
      "s_waitcnt lgkmcnt(5)\n\t"
      "v_mfma_f32_16x16x32_bf16 v[54:57], v[134:137], v[214:217], 0\n\t"
      "ds_read_b128 v[86:89], v238 offset:2560\n\t"
      "v_mfma_f32_16x16x32_bf16 v[58:61], v[138:141], v[218:221], 0\n\t"
      "ds_read_b32 v118, v182 offset:2048\n\t"
      "v_mfma_f32_16x16x32_bf16 v[62:65], v[142:145], v[222:225], 0\n\t"
      "ds_read_b128 v[90:93], v239 offset:2560\n\t"
      "s_waitcnt lgkmcnt(7)\n\t"
      "v_mfma_f32_16x16x32_bf16 v[66:69], v[146:149], v[226:229], 0\n\t"
      "ds_read_b32 v119, v183 offset:2048\n\t"
      "v_smfmac_f32_16x16x64_bf16 v[54:57], a[0:3], v[102:109], v6\n\t"
      "ds_read_b128 v[94:97], v240 offset:2560\n\t"
      "v_smfmac_f32_16x16x64_bf16 v[54:57], a[16:19], v[110:117], v8\n\t"
      "ds_read_b32 v120, v184 offset:2048\n\t"
      "v_smfmac_f32_16x16x64_bf16 v[58:61], a[4:7], v[102:109], v6 abid:1\n\t"
      "ds_read_b128 v[98:101], v241 offset:2560\n\t"
      "v_smfmac_f32_16x16x64_bf16 v[58:61], a[20:23], v[110:117], v8 abid:1\n\t"
      "ds_read_b32 v121, v185 offset:2048\n\t"
      "v_smfmac_f32_16x16x64_bf16 v[62:65], a[8:11], v[102:109], v7\n\t"
      "ds_read_b32 v122, v186 offset:2048\n\t"
      "v_smfmac_f32_16x16x64_bf16 v[62:65], a[24:27], v[110:117], v9\n\t"
      "ds_read_b32 v123, v187 offset:2048\n\t"
      "v_smfmac_f32_16x16x64_bf16 v[66:69], a[12:15], v[102:109], v7 abid:1\n\t"
      "s_waitcnt lgkmcnt(8)\n\t"
      "ds_read_b32 v124, v188 offset:2048\n\t"
      "v_smfmac_f32_16x16x64_bf16 v[66:69], a[28:31], v[110:117], v9 abid:1\n\t"
      "ds_read_b32 v125, v189 offset:2048\n\t"
      "v_smfmac_f32_16x16x64_bf16 v[54:57], a[32:35], v[70:77], v10\n\t"
      "ds_read_b128 v[102:105], v238 offset:2816\n\t"
      "v_cvt_pk_bf16_f32 v38, v38, v39\n\t"
      "v_smfmac_f32_16x16x64_bf16 v[54:57], a[48:51], v[78:85], v12\n\t"
      "ds_read_b32 v126, v190 offset:2048\n\t"
      "v_cvt_pk_bf16_f32 v39, v40, v41\n\t"
      "v_smfmac_f32_16x16x64_bf16 v[58:61], a[36:39], v[70:77], v10 abid:1\n\t"
      "ds_read_b128 v[106:109], v239 offset:2816\n\t"
      "v_cvt_pk_bf16_f32 v40, v42, v43\n\t"
      "v_smfmac_f32_16x16x64_bf16 v[58:61], a[52:55], v[78:85], v12 abid:1\n\t"
      "ds_read_b32 v127, v191 offset:2048\n\t"
      "v_cvt_pk_bf16_f32 v41, v44, v45\n\t"
      "v_smfmac_f32_16x16x64_bf16 v[62:65], a[40:43], v[70:77], v11\n\t"
      "s_waitcnt lgkmcnt(8)\n\t"
      "ds_read_b128 v[110:113], v240 offset:2816\n\t"
      "v_cvt_pk_bf16_f32 v46, v46, v47\n\t"
      "v_smfmac_f32_16x16x64_bf16 v[62:65], a[56:59], v[78:85], v13\n\t"
      "ds_read_b32 v128, v192 offset:2048\n\t"
      "v_cvt_pk_bf16_f32 v47, v48, v49\n\t"
      "v_smfmac_f32_16x16x64_bf16 v[66:69], a[44:47], v[70:77], v11 abid:1\n\t"
      "ds_read_b128 v[114:117], v241 offset:2816\n\t"
      "v_cvt_pk_bf16_f32 v48, v50, v51\n\t"
      "v_smfmac_f32_16x16x64_bf16 v[66:69], a[60:63], v[78:85], v13 abid:1\n\t"
      "ds_read_b32 v129, v193 offset:2048\n\t"
      "v_cvt_pk_bf16_f32 v49, v52, v53\n\t"
      "v_smfmac_f32_16x16x64_bf16 v[54:57], a[64:67], v[86:93], v14\n\t"
      "ds_read_b128 v[70:73], v238 offset:3072\n\t"
      "v_mov_b32_dpp v42, v46 row_ror:8 row_mask:0xf bank_mask:0xf\n\t"
      "v_smfmac_f32_16x16x64_bf16 v[54:57], a[80:83], v[94:101], v16\n\t"
      "ds_read_b32 v130, v194 offset:2048\n\t"
      "v_mov_b32_dpp v43, v47 row_ror:8 row_mask:0xf bank_mask:0xf\n\t"
      "v_smfmac_f32_16x16x64_bf16 v[58:61], a[68:71], v[86:93], v14 abid:1\n\t"
      "s_waitcnt lgkmcnt(8)\n\t"
      "ds_read_b128 v[74:77], v239 offset:3072\n\t"
      "v_mov_b32_dpp v44, v48 row_ror:8 row_mask:0xf bank_mask:0xf\n\t"
      "v_smfmac_f32_16x16x64_bf16 v[58:61], a[84:87], v[94:101], v16 abid:1\n\t"
      "ds_read_b32 v131, v195 offset:2048\n\t"
      "v_mov_b32_dpp v45, v49 row_ror:8 row_mask:0xf bank_mask:0xf\n\t"
      "v_smfmac_f32_16x16x64_bf16 v[62:65], a[72:75], v[86:93], v15\n\t"
      "ds_read_b128 v[78:81], v240 offset:3072\n\t"
      "v_mov_b32_dpp v46, v38 row_ror:8 row_mask:0xf bank_mask:0x3\n\t"
      "v_smfmac_f32_16x16x64_bf16 v[62:65], a[88:91], v[94:101], v17\n\t"
      "ds_read_b32 v132, v196 offset:2048\n\t"
      "v_mov_b32_dpp v47, v39 row_ror:8 row_mask:0xf bank_mask:0x3\n\t"
      "v_smfmac_f32_16x16x64_bf16 v[66:69], a[76:79], v[86:93], v15 abid:1\n\t"
      "ds_read_b128 v[82:85], v241 offset:3072\n\t"
      "v_mov_b32_dpp v48, v40 row_ror:8 row_mask:0xf bank_mask:0x3\n\t"
      "v_smfmac_f32_16x16x64_bf16 v[66:69], a[92:95], v[94:101], v17 abid:1\n\t"
      "ds_read_b32 v133, v197 offset:2048\n\t"
      "v_mov_b32_dpp v49, v41 row_ror:8 row_mask:0xf bank_mask:0x3\n\t"
      "s_waitcnt vmcnt(12)\n\t"
      "s_barrier\n\t"
      "s_mov_b32 s71, s67\n\t"
      "s_waitcnt lgkmcnt(9)\n\t"
      "v_smfmac_f32_16x16x64_bf16 v[54:57], a[96:99], v[102:109], v18\n\t"
      "ds_read_b128 v[86:89], v238 offset:3328\n\t"
      "v_mov_b32_dpp v38, v42 quad_perm:[0,1,2,3] row_mask:0xf bank_mask:0xc\n\t"
      "s_add_u32 m0, s46, s71\n\t"
      "s_nop 0\n\t"
      "global_load_lds_dwordx4 %11, s[36:37]\n\t"
      "v_smfmac_f32_16x16x64_bf16 v[54:57], a[112:115], v[110:117], v20\n\t"
      "ds_read_b128 v[90:93], v239 offset:3328\n\t"
      "v_mov_b32_dpp v39, v43 quad_perm:[0,1,2,3] row_mask:0xf bank_mask:0xc\n\t"
      "v_smfmac_f32_16x16x64_bf16 v[58:61], a[100:103], v[102:109], v18 abid:1\n\t"
      "ds_read_b128 v[94:97], v240 offset:3328\n\t"
      "v_mov_b32_dpp v40, v44 quad_perm:[0,1,2,3] row_mask:0xf bank_mask:0xc\n\t"
      "v_smfmac_f32_16x16x64_bf16 v[58:61], a[116:119], v[110:117], v20 abid:1\n\t"
      "ds_read_b128 v[98:101], v241 offset:3328\n\t"
      "v_mov_b32_dpp v41, v45 quad_perm:[0,1,2,3] row_mask:0xf bank_mask:0xc\n\t"
      "global_load_lds_dwordx4 %11, s[36:37] offset:1024\n\t"
      "v_smfmac_f32_16x16x64_bf16 v[62:65], a[104:107], v[102:109], v19\n\t"
      "v_xor_b32 v166, s57, v166\n\t"
      "v_smfmac_f32_16x16x64_bf16 v[62:65], a[120:123], v[110:117], v21\n\t"
      "v_xor_b32 v167, s57, v167\n\t"
      "v_smfmac_f32_16x16x64_bf16 v[66:69], a[108:111], v[102:109], v19 abid:1\n\t"
      "v_xor_b32 v168, s57, v168\n\t"
      "s_add_u32 m0, s47, s71\n\t"
      "s_nop 0\n\t"
      "global_load_lds_dwordx4 %12, s[36:37]\n\t"
      "v_smfmac_f32_16x16x64_bf16 v[66:69], a[124:127], v[110:117], v21 abid:1\n\t"
      "v_xor_b32 v169, s57, v169\n\t"
      "v_xor_b32 v170, s57, v170\n\t"
      "v_xor_b32 v171, s57, v171\n\t"
      "v_xor_b32 v172, s57, v172\n\t"
      "v_xor_b32 v173, s57, v173\n\t"
      "v_xor_b32 v174, s57, v174\n\t"
      "v_xor_b32 v175, s57, v175\n\t"
      "v_xor_b32 v176, s57, v176\n\t"
      "v_xor_b32 v177, s57, v177\n\t"
      "v_xor_b32 v178, s57, v178\n\t"
      "v_xor_b32 v179, s57, v179\n\t"
      "v_xor_b32 v180, s57, v180\n\t"
      "v_xor_b32 v181, s57, v181\n\t"
      "s_waitcnt lgkmcnt(5)\n\t"
      "v_mfma_f32_16x16x32_bf16 v[54:57], v[150:153], v[118:121], v[54:57]\n\t"
      "ds_read_b128 v[102:105], v238 offset:3584\n\t"
      "v_mfma_f32_16x16x32_bf16 v[58:61], v[154:157], v[122:125], v[58:61]\n\t"
      "ds_read_b32 v214, v166 offset:0\n\t"
      "global_load_lds_dwordx4 %12, s[36:37] offset:1024\n\t"
      "v_mfma_f32_16x16x32_bf16 v[62:65], v[158:161], v[126:129], v[62:65]\n\t"
      "ds_read_b128 v[106:109], v239 offset:3584\n\t"
      "s_waitcnt lgkmcnt(7)\n\t"
      "v_mfma_f32_16x16x32_bf16 v[66:69], v[162:165], v[130:133], v[66:69]\n\t"
      "ds_read_b32 v215, v167 offset:0\n\t"
      "v_smfmac_f32_16x16x64_bf16 v[54:57], a[128:131], v[70:77], v22\n\t"
      "ds_read_b128 v[110:113], v240 offset:3584\n\t"
      "s_add_u32 m0, s48, s71\n\t"
      "s_nop 0\n\t"
      "global_load_lds_dwordx4 %13, s[36:37]\n\t"
      "v_smfmac_f32_16x16x64_bf16 v[54:57], a[144:147], v[78:85], v24\n\t"
      "ds_read_b32 v216, v168 offset:0\n\t"
      "v_smfmac_f32_16x16x64_bf16 v[58:61], a[132:135], v[70:77], v22 abid:1\n\t"
      "ds_read_b128 v[114:117], v241 offset:3584\n\t"
      "v_smfmac_f32_16x16x64_bf16 v[58:61], a[148:151], v[78:85], v24 abid:1\n\t"
      "s_mov_b64 exec, s[68:69]\n\t"
      "ds_read_b32 v217, v169 offset:0\n\t"
      "s_mov_b64 exec, -1\n\t"
      "global_load_lds_dwordx4 %13, s[36:37] offset:1024\n\t"
      "v_smfmac_f32_16x16x64_bf16 v[62:65], a[136:139], v[70:77], v23\n\t"
      "ds_read_b32 v218, v170 offset:0\n\t"
      "v_smfmac_f32_16x16x64_bf16 v[62:65], a[152:155], v[78:85], v25\n\t"
      "ds_read_b32 v219, v171 offset:0\n\t"
      "v_smfmac_f32_16x16x64_bf16 v[66:69], a[140:143], v[70:77], v23 abid:1\n\t"
      "s_waitcnt lgkmcnt(8)\n\t"
      "ds_read_b32 v220, v172 offset:0\n\t"
      "s_add_u32 m0, s49, s71\n\t"
      "s_nop 0\n\t"
      "global_load_lds_dwordx4 %14, s[36:37]\n\t"
      "v_smfmac_f32_16x16x64_bf16 v[66:69], a[156:159], v[78:85], v25 abid:1\n\t"
      "s_mov_b64 exec, s[68:69]\n\t"
      "ds_read_b32 v221, v173 offset:0\n\t"
      "s_mov_b64 exec, -1\n\t"
      "v_smfmac_f32_16x16x64_bf16 v[54:57], a[160:163], v[86:93], v26\n\t"
      "ds_read_b32 v222, v174 offset:0\n\t"
      "v_xor_b32 v182, s57, v182\n\t"
      "v_smfmac_f32_16x16x64_bf16 v[54:57], a[176:179], v[94:101], v28\n\t"
      "ds_read_b32 v223, v175 offset:0\n\t"
      "v_xor_b32 v183, s57, v183\n\t"
      "global_load_lds_dwordx4 %14, s[36:37] offset:1024\n\t"
      "s_cmp_lt_u32 s66, 5\n\t"
      "s_cselect_b32 s55, s73, 0\n\t"
      "s_add_u32 s36, s36, s73\n\t"
      "s_addc_u32 s37, s37, 0\n\t"
      "s_mov_b32 s73, s55\n\t"
      "v_smfmac_f32_16x16x64_bf16 v[58:61], a[164:167], v[86:93], v26 abid:1\n\t"
      "ds_read_b32 v224, v176 offset:0\n\t"
      "v_xor_b32 v184, s57, v184\n\t"
      "v_smfmac_f32_16x16x64_bf16 v[58:61], a[180:183], v[94:101], v28 abid:1\n\t"
      "s_mov_b64 exec, s[68:69]\n\t"
      "ds_read_b32 v225, v177 offset:0\n\t"
      "s_mov_b64 exec, -1\n\t"
      "v_xor_b32 v185, s57, v185\n\t"
      "v_smfmac_f32_16x16x64_bf16 v[62:65], a[168:171], v[86:93], v27\n\t"
      "s_waitcnt lgkmcnt(8)\n\t"
      "ds_read_b32 v226, v178 offset:0\n\t"
      "v_xor_b32 v186, s57, v186\n\t"
      "v_smfmac_f32_16x16x64_bf16 v[62:65], a[184:187], v[94:101], v29\n\t"
      "ds_read_b32 v227, v179 offset:0\n\t"
      "v_xor_b32 v187, s57, v187\n\t"
      "v_smfmac_f32_16x16x64_bf16 v[66:69], a[172:175], v[86:93], v27 abid:1\n\t"
      "ds_read_b32 v228, v180 offset:0\n\t"
      "v_xor_b32 v188, s57, v188\n\t"
      "v_smfmac_f32_16x16x64_bf16 v[66:69], a[188:191], v[94:101], v29 abid:1\n\t"
      "s_mov_b64 exec, s[68:69]\n\t"
      "ds_read_b32 v229, v181 offset:0\n\t"
      "s_mov_b64 exec, -1\n\t"
      "v_xor_b32 v189, s57, v189\n\t"
      "ds_read_b128 v[86:89], v238 offset:3840\n\t"
      "ds_read_b128 v[90:93], v239 offset:3840\n\t"
      "s_waitcnt lgkmcnt(8)\n\t"
      "ds_read_b128 v[94:97], v240 offset:3840\n\t"
      "ds_read_b128 v[98:101], v241 offset:3840\n\t"
      "v_xor_b32 v238, s57, v238\n\t"
      "v_xor_b32 v239, s57, v239\n\t"
      "v_xor_b32 v240, s57, v240\n\t"
      "v_xor_b32 v241, s57, v241\n\t"
      "v_smfmac_f32_16x16x64_bf16 v[54:57], a[192:195], v[102:109], v30\n\t"
      "ds_read_b128 v[70:73], v238 offset:0\n\t"
      "v_xor_b32 v190, s57, v190\n\t"
      "v_smfmac_f32_16x16x64_bf16 v[54:57], a[208:211], v[110:117], v32\n\t"
      "ds_read_b128 v[74:77], v239 offset:0\n\t"
      "v_xor_b32 v191, s57, v191\n\t"
      "v_smfmac_f32_16x16x64_bf16 v[58:61], a[196:199], v[102:109], v30 abid:1\n\t"
      "ds_read_b128 v[78:81], v240 offset:0\n\t"
      "v_xor_b32 v192, s57, v192\n\t"
      "v_smfmac_f32_16x16x64_bf16 v[58:61], a[212:215], v[110:117], v32 abid:1\n\t"
      "ds_read_b128 v[82:85], v241 offset:0\n\t"
      "v_xor_b32 v193, s57, v193\n\t"
      "v_smfmac_f32_16x16x64_bf16 v[62:65], a[200:203], v[102:109], v31\n\t"
      "v_xor_b32 v194, s57, v194\n\t"
      "v_smfmac_f32_16x16x64_bf16 v[62:65], a[216:219], v[110:117], v33\n\t"
      "v_xor_b32 v195, s57, v195\n\t"
      "v_smfmac_f32_16x16x64_bf16 v[66:69], a[204:207], v[102:109], v31 abid:1\n\t"
      "v_xor_b32 v196, s57, v196\n\t"
      "v_smfmac_f32_16x16x64_bf16 v[66:69], a[220:223], v[110:117], v33 abid:1\n\t"
      "v_xor_b32 v197, s57, v197\n\t"
      "s_waitcnt lgkmcnt(4)\n\t"
      "v_smfmac_f32_16x16x64_bf16 v[54:57], a[224:227], v[86:93], v34\n\t"
      "global_store_dwordx4 v244, v[38:41], s[38:39] sc1\n\t"
      "v_smfmac_f32_16x16x64_bf16 v[54:57], a[240:243], v[94:101], v36\n\t"
      "v_smfmac_f32_16x16x64_bf16 v[58:61], a[228:231], v[86:93], v34 abid:1\n\t"
      "global_store_dwordx4 v252, v[46:49], s[38:39] sc1\n\t"
      "v_smfmac_f32_16x16x64_bf16 v[58:61], a[244:247], v[94:101], v36 abid:1\n\t"
      "v_smfmac_f32_16x16x64_bf16 v[62:65], a[232:235], v[86:93], v35\n\t"
      "s_add_u32 s38, s38, 2097152\n\t"
      "s_addc_u32 s39, s39, 0\n\t"
      "v_smfmac_f32_16x16x64_bf16 v[62:65], a[248:251], v[94:101], v37\n\t"
      "v_smfmac_f32_16x16x64_bf16 v[66:69], a[236:239], v[86:93], v35 abid:1\n\t"
      "v_smfmac_f32_16x16x64_bf16 v[66:69], a[252:255], v[94:101], v37 abid:1\n\t"
      "s_xor_b32 s67, s67, 4096\n\t"
      "s_add_u32 s66, s66, 1\n\t"
      "s_cmp_lt_u32 s66, 8\n\t"
      "s_cbranch_scc1 .LloopA%=\n\t"
      ".Lfin%=:\n\t"
      "s_nop 4\n\t"
      "v_cvt_pk_bf16_f32 v54, v54, v55\n\t"
      "v_cvt_pk_bf16_f32 v55, v56, v57\n\t"
      "v_cvt_pk_bf16_f32 v56, v58, v59\n\t"
      "v_cvt_pk_bf16_f32 v57, v60, v61\n\t"
      "v_cvt_pk_bf16_f32 v62, v62, v63\n\t"
      "v_cvt_pk_bf16_f32 v63, v64, v65\n\t"
      "v_cvt_pk_bf16_f32 v64, v66, v67\n\t"
      "v_cvt_pk_bf16_f32 v65, v68, v69\n\t"
      "s_nop 1\n\t"
      "v_mov_b32_dpp v58, v62 row_ror:8 row_mask:0xf bank_mask:0xf\n\t"
      "v_mov_b32_dpp v59, v63 row_ror:8 row_mask:0xf bank_mask:0xf\n\t"
      "v_mov_b32_dpp v60, v64 row_ror:8 row_mask:0xf bank_mask:0xf\n\t"
      "v_mov_b32_dpp v61, v65 row_ror:8 row_mask:0xf bank_mask:0xf\n\t"
      "v_mov_b32_dpp v62, v54 row_ror:8 row_mask:0xf bank_mask:0x3\n\t"
      "v_mov_b32_dpp v63, v55 row_ror:8 row_mask:0xf bank_mask:0x3\n\t"
      "v_mov_b32_dpp v64, v56 row_ror:8 row_mask:0xf bank_mask:0x3\n\t"
      "v_mov_b32_dpp v65, v57 row_ror:8 row_mask:0xf bank_mask:0x3\n\t"
      "v_mov_b32_dpp v54, v58 quad_perm:[0,1,2,3] row_mask:0xf bank_mask:0xc\n\t"
      "v_mov_b32_dpp v55, v59 quad_perm:[0,1,2,3] row_mask:0xf bank_mask:0xc\n\t"
      "v_mov_b32_dpp v56, v60 quad_perm:[0,1,2,3] row_mask:0xf bank_mask:0xc\n\t"
      "v_mov_b32_dpp v57, v61 quad_perm:[0,1,2,3] row_mask:0xf bank_mask:0xc\n\t"
      "global_store_dwordx4 v244, v[54:57], s[38:39] sc1\n\t"
      "global_store_dwordx4 v252, v[62:65], s[38:39] sc1\n\t"
      "s_or_b32 s55, %10, %1\n\t"
      "s_cmp_eq_u32 s55, 0\n\t"
      "s_cbranch_scc1 .Lep%=\n\t"
      ".Lnoep%=:\n\t"
      "s_waitcnt vmcnt(0) lgkmcnt(0)\n\t"
      "s_branch .Lend%=\n\t"
      ".LverB%=:\n\t"
      "v_lshlrev_b32 v249, 6, %0\n\t"
      "s_getpc_b64 s[60:61]\n\t"
      "s_waitcnt vmcnt(61)\n\t"
      "global_load_dword v251, v249, s[60:61]\n\t"
      "s_add_u32 s60, s60, 4096\n\t"
      "s_addc_u32 s61, s61, 0\n\t"
      "s_waitcnt vmcnt(61)\n\t"
      "global_load_dword v251, v249, s[60:61]\n\t"
      "s_add_u32 s60, s60, 4096\n\t"
      "s_addc_u32 s61, s61, 0\n\t"
      "v_add_u32 v250, 0, v247\n\t"
      "v_xor_b32_sdwa v206, v246, v198 dst_sel:DWORD dst_unused:UNUSED_PAD src0_sel:DWORD src1_sel:WORD_0\n\t"
      "v_mad_u32_u16 v198, v198, s58, v250 op_sel:[1,0,0,0]\n\t"
      "s_waitcnt vmcnt(61)\n\t"
      "global_load_dword v198, v198, s[42:43] sc1\n\t"
      "v_xor_b32_sdwa v207, v246, v199 dst_sel:DWORD dst_unused:UNUSED_PAD src0_sel:DWORD src1_sel:WORD_0\n\t"
      "v_mad_u32_u16 v199, v199, s58, v250 op_sel:[1,0,0,0]\n\t"
      "s_waitcnt vmcnt(61)\n\t"
      "global_load_dword v199, v199, s[42:43] sc1\n\t"
      "v_add_u32 v250, 32896, v247\n\t"
      "v_xor_b32_sdwa v208, v246, v200 dst_sel:DWORD dst_unused:UNUSED_PAD src0_sel:DWORD src1_sel:WORD_0\n\t"
      "v_mad_u32_u16 v200, v200, s58, v250 op_sel:[1,0,0,0]\n\t"
      "s_waitcnt vmcnt(61)\n\t"
      "global_load_dword v200, v200, s[42:43] sc1\n\t"
      "v_xor_b32_sdwa v209, v246, v201 dst_sel:DWORD dst_unused:UNUSED_PAD src0_sel:DWORD src1_sel:WORD_0\n\t"
      "v_mad_u32_u16 v201, v201, s58, v250 op_sel:[1,0,0,0]\n\t"
      "s_waitcnt vmcnt(61)\n\t"
      "global_load_dword v201, v201, s[42:43] sc1\n\t"
      "v_add_u32 v250, 65792, v247\n\t"
      "v_xor_b32_sdwa v210, v246, v202 dst_sel:DWORD dst_unused:UNUSED_PAD src0_sel:DWORD src1_sel:WORD_0\n\t"
      "v_mad_u32_u16 v202, v202, s58, v250 op_sel:[1,0,0,0]\n\t"
      "s_waitcnt vmcnt(61)\n\t"
      "global_load_dword v202, v202, s[42:43] sc1\n\t"
      "v_xor_b32_sdwa v211, v246, v203 dst_sel:DWORD dst_unused:UNUSED_PAD src0_sel:DWORD src1_sel:WORD_0\n\t"
      "v_mad_u32_u16 v203, v203, s58, v250 op_sel:[1,0,0,0]\n\t"
      "s_waitcnt vmcnt(61)\n\t"
      "global_load_dword v203, v203, s[42:43] sc1\n\t"
      "v_add_u32 v250, 98688, v247\n\t"
      "v_xor_b32_sdwa v212, v246, v204 dst_sel:DWORD dst_unused:UNUSED_PAD src0_sel:DWORD src1_sel:WORD_0\n\t"
      "v_mad_u32_u16 v204, v204, s58, v250 op_sel:[1,0,0,0]\n\t"
      "s_waitcnt vmcnt(61)\n\t"
      "global_load_dword v204, v204, s[42:43] sc1\n\t"
      "v_xor_b32_sdwa v213, v246, v205 dst_sel:DWORD dst_unused:UNUSED_PAD src0_sel:DWORD src1_sel:WORD_0\n\t"
      "v_mad_u32_u16 v205, v205, s58, v250 op_sel:[1,0,0,0]\n\t"
      "s_waitcnt vmcnt(61)\n\t"
      "global_load_dword v205, v205, s[42:43] sc1\n\t"
      "v_smfmac_f32_16x16x64_bf16 v[38:41], a[32:35], v[70:77], v10\n\t"
      "ds_read_b128 v[102:105], v238 offset:512\n\t"
      "v_smfmac_f32_16x16x64_bf16 v[38:41], a[48:51], v[78:85], v12\n\t"
      "ds_read_b32 v214, v166 offset:0\n\t"
      "v_smfmac_f32_16x16x64_bf16 v[42:45], a[36:39], v[70:77], v10 abid:1\n\t"
      "ds_read_b128 v[106:109], v239 offset:512\n\t"
      "s_add_u32 s40, s40, 4096\n\t"
      "s_addc_u32 s41, s41, 0\n\t"
      "s_waitcnt vmcnt(61)\n\t"
      "global_load_dwordx4 a[140:143], v245, s[40:41] sc1\n\t"
      "v_smfmac_f32_16x16x64_bf16 v[42:45], a[52:55], v[78:85], v12 abid:1\n\t"
      "ds_read_b32 v215, v167 offset:0\n\t"
      "v_smfmac_f32_16x16x64_bf16 v[46:49], a[40:43], v[70:77], v11\n\t"
      "ds_read_b128 v[110:113], v240 offset:512\n\t"
      "v_smfmac_f32_16x16x64_bf16 v[46:49], a[56:59], v[78:85], v13\n\t"
      "ds_read_b32 v216, v168 offset:0\n\t"
      "s_waitcnt vmcnt(61)\n\t"
      "global_load_dwordx4 a[144:147], v245, s[40:41] offset:1024 sc1\n\t"
      "v_smfmac_f32_16x16x64_bf16 v[50:53], a[44:47], v[70:77], v11 abid:1\n\t"
      "ds_read_b128 v[114:117], v241 offset:512\n\t"
      "v_smfmac_f32_16x16x64_bf16 v[50:53], a[60:63], v[78:85], v13 abid:1\n\t"
      "s_mov_b64 exec, s[68:69]\n\t"
      "ds_read_b32 v217, v169 offset:0\n\t"
      "s_mov_b64 exec, -1\n\t"
      "v_smfmac_f32_16x16x64_bf16 v[54:57], a[32:35], v[86:93], v10\n\t"
      "ds_read_b128 v[70:73], v238 offset:2560\n\t"
      "s_waitcnt vmcnt(61)\n\t"
      "global_load_dwordx4 a[148:151], v245, s[40:41] offset:2048 sc1\n\t"
      "v_smfmac_f32_16x16x64_bf16 v[54:57], a[48:51], v[94:101], v12\n\t"
      "ds_read_b32 v218, v170 offset:0\n\t"
      "v_smfmac_f32_16x16x64_bf16 v[58:61], a[36:39], v[86:93], v10 abid:1\n\t"
      "ds_read_b128 v[74:77], v239 offset:2560\n\t"
      "v_smfmac_f32_16x16x64_bf16 v[58:61], a[52:55], v[94:101], v12 abid:1\n\t"
      "ds_read_b32 v219, v171 offset:0\n\t"
      "s_waitcnt vmcnt(61)\n\t"
      "global_load_dwordx4 a[152:155], v245, s[40:41] offset:3072 sc1\n\t"
      "v_smfmac_f32_16x16x64_bf16 v[62:65], a[40:43], v[86:93], v11\n\t"
      "ds_read_b128 v[78:81], v240 offset:2560\n\t"
      "v_smfmac_f32_16x16x64_bf16 v[62:65], a[56:59], v[94:101], v13\n\t"
      "ds_read_b32 v220, v172 offset:0\n\t"
      "v_smfmac_f32_16x16x64_bf16 v[66:69], a[44:47], v[86:93], v11 abid:1\n\t"
      "s_waitcnt lgkmcnt(8)\n\t"
      "ds_read_b128 v[82:85], v241 offset:2560\n\t"
      "s_add_u32 s40, s40, 4096\n\t"
      "s_addc_u32 s41, s41, 0\n\t"
      "s_waitcnt vmcnt(61)\n\t"
      "global_load_dwordx4 a[156:159], v245, s[40:41] sc1\n\t"
      "v_smfmac_f32_16x16x64_bf16 v[66:69], a[60:63], v[94:101], v13 abid:1\n\t"
      "s_mov_b64 exec, s[68:69]\n\t"
      "ds_read_b32 v221, v173 offset:0\n\t"
      "s_mov_b64 exec, -1\n\t"
      "s_waitcnt lgkmcnt(9)\n\t"
      "v_smfmac_f32_16x16x64_bf16 v[38:41], a[64:67], v[102:109], v14\n\t"
      "ds_read_b128 v[86:89], v238 offset:768\n\t"
      "v_smfmac_f32_16x16x64_bf16 v[38:41], a[80:83], v[110:117], v16\n\t"
      "ds_read_b32 v222, v174 offset:0\n\t"
      "s_waitcnt vmcnt(61)\n\t"
      "global_load_dwordx2 v[26:27], v252, s[40:41] offset:1024 sc1\n\t"
      "s_waitcnt vmcnt(61)\n\t"
      "global_load_dwordx2 v[28:29], v252, s[40:41] offset:1536 sc1\n\t"
      "v_smfmac_f32_16x16x64_bf16 v[42:45], a[68:71], v[102:109], v14 abid:1\n\t"
      "ds_read_b128 v[90:93], v239 offset:768\n\t"
      "v_smfmac_f32_16x16x64_bf16 v[42:45], a[84:87], v[110:117], v16 abid:1\n\t"
      "ds_read_b32 v223, v175 offset:0\n\t"
      "v_smfmac_f32_16x16x64_bf16 v[46:49], a[72:75], v[102:109], v15\n\t"
      "ds_read_b128 v[94:97], v240 offset:768\n\t"
      "s_waitcnt vmcnt(61)\n\t"
      "global_load_dwordx4 a[160:163], v245, s[40:41] offset:2048 sc1\n\t"
      "v_smfmac_f32_16x16x64_bf16 v[46:49], a[88:91], v[110:117], v17\n\t"
      "s_waitcnt lgkmcnt(8)\n\t"
      "ds_read_b32 v224, v176 offset:0\n\t"
      "v_smfmac_f32_16x16x64_bf16 v[50:53], a[76:79], v[102:109], v15 abid:1\n\t"
      "ds_read_b128 v[98:101], v241 offset:768\n\t"
      "v_smfmac_f32_16x16x64_bf16 v[50:53], a[92:95], v[110:117], v17 abid:1\n\t"
      "s_mov_b64 exec, s[68:69]\n\t"
      "ds_read_b32 v225, v177 offset:0\n\t"
      "s_mov_b64 exec, -1\n\t"
      "s_waitcnt vmcnt(61)\n\t"
      "global_load_dwordx4 a[164:167], v245, s[40:41] offset:3072 sc1\n\t"
      "s_waitcnt lgkmcnt(9)\n\t"
      "v_smfmac_f32_16x16x64_bf16 v[54:57], a[64:67], v[70:77], v14\n\t"
      "ds_read_b128 v[102:105], v238 offset:2816\n\t"
      "v_smfmac_f32_16x16x64_bf16 v[54:57], a[80:83], v[78:85], v16\n\t"
      "ds_read_b32 v226, v178 offset:0\n\t"
      "v_smfmac_f32_16x16x64_bf16 v[58:61], a[68:71], v[70:77], v14 abid:1\n\t"
      "ds_read_b128 v[106:109], v239 offset:2816\n\t"
      "s_add_u32 s40, s40, 4096\n\t"
      "s_addc_u32 s41, s41, 0\n\t"
      "s_waitcnt vmcnt(61)\n\t"
      "global_load_dwordx4 a[168:171], v245, s[40:41] sc1\n\t"
      "v_smfmac_f32_16x16x64_bf16 v[58:61], a[84:87], v[78:85], v16 abid:1\n\t"
      "ds_read_b32 v227, v179 offset:0\n\t"
      "v_smfmac_f32_16x16x64_bf16 v[62:65], a[72:75], v[70:77], v15\n\t"
      "ds_read_b128 v[110:113], v240 offset:2816\n\t"
      "v_smfmac_f32_16x16x64_bf16 v[62:65], a[88:91], v[78:85], v17\n\t"
      "s_waitcnt lgkmcnt(8)\n\t"
      "ds_read_b32 v228, v180 offset:0\n\t"
      "s_waitcnt vmcnt(61)\n\t"
      "global_load_dwordx4 a[172:175], v245, s[40:41] offset:1024 sc1\n\t"
      "v_smfmac_f32_16x16x64_bf16 v[66:69], a[76:79], v[70:77], v15 abid:1\n\t"
      "ds_read_b128 v[114:117], v241 offset:2816\n\t"
      "v_smfmac_f32_16x16x64_bf16 v[66:69], a[92:95], v[78:85], v17 abid:1\n\t"
      "s_mov_b64 exec, s[68:69]\n\t"
      "ds_read_b32 v229, v181 offset:0\n\t"
      "s_mov_b64 exec, -1\n\t"
      "s_waitcnt vmcnt(58)\n\t"
      "s_waitcnt lgkmcnt(9)\n\t"
      "v_smfmac_f32_16x16x64_bf16 v[38:41], a[96:99], v[86:93], v18\n\t"
      "ds_read_b128 v[70:73], v238 offset:1024\n\t"
      "global_load_dwordx4 a[176:179], v245, s[40:41] offset:2048 sc1\n\t"
      "v_smfmac_f32_16x16x64_bf16 v[38:41], a[112:115], v[94:101], v20\n\t"
      "ds_read_b32 v118, v182 offset:0\n\t"
      "v_smfmac_f32_16x16x64_bf16 v[42:45], a[100:103], v[86:93], v18 abid:1\n\t"
      "ds_read_b128 v[74:77], v239 offset:1024\n\t"
      "v_smfmac_f32_16x16x64_bf16 v[42:45], a[116:119], v[94:101], v20 abid:1\n\t"
      "ds_read_b32 v119, v183 offset:0\n\t"
      "global_load_dwordx4 a[180:183], v245, s[40:41] offset:3072 sc1\n\t"
      "v_smfmac_f32_16x16x64_bf16 v[46:49], a[104:107], v[86:93], v19\n\t"
      "ds_read_b128 v[78:81], v240 offset:1024\n\t"
      "v_smfmac_f32_16x16x64_bf16 v[46:49], a[120:123], v[94:101], v21\n\t"
      "s_waitcnt lgkmcnt(8)\n\t"
      "ds_read_b32 v120, v184 offset:0\n\t"
      "v_smfmac_f32_16x16x64_bf16 v[50:53], a[108:111], v[86:93], v19 abid:1\n\t"
      "ds_read_b128 v[82:85], v241 offset:1024\n\t"
      "s_add_u32 s40, s40, 4096\n\t"
      "s_addc_u32 s41, s41, 0\n\t"
      "global_load_dwordx4 a[184:187], v245, s[40:41] sc1\n\t"
      "v_smfmac_f32_16x16x64_bf16 v[50:53], a[124:127], v[94:101], v21 abid:1\n\t"
      "ds_read_b32 v121, v185 offset:0\n\t"
      "s_waitcnt vmcnt(14)\n\t"
      "s_waitcnt lgkmcnt(9)\n\t"
      "v_mfma_f32_16x16x32_bf16 v[38:41], v[134:137], v[214:217], v[38:41]\n\t"
      "ds_read_b128 v[86:89], v238 offset:3072\n\t"
      "v_mfma_f32_16x16x32_bf16 v[42:45], v[138:141], v[218:221], v[42:45]\n\t"
      "ds_read_b32 v122, v186 offset:0\n\t"
      "global_load_dwordx4 a[188:191], v245, s[40:41] offset:1024 sc1\n\t"
      "v_mfma_f32_16x16x32_bf16 v[46:49], v[142:145], v[222:225], v[46:49]\n\t"
      "ds_read_b128 v[90:93], v239 offset:3072\n\t"
      "s_waitcnt lgkmcnt(11)\n\t"
      "v_mfma_f32_16x16x32_bf16 v[50:53], v[146:149], v[226:229], v[50:53]\n\t"
      "ds_read_b32 v123, v187 offset:0\n\t"
      "s_waitcnt lgkmcnt(4)\n\t"
      "v_mfma_f32_16x16x32_bf16 v[38:41], v[150:153], v[118:121], v[38:41]\n\t"
      "ds_read_b128 v[94:97], v240 offset:3072\n\t"
      "global_load_dwordx2 v[30:31], v252, s[40:41] offset:2048 sc1\n\t"
      "global_load_dwordx2 v[32:33], v252, s[40:41] offset:2560 sc1\n\t"
      "ds_read_b32 v124, v188 offset:0\n\t"
      "ds_read_b32 v125, v189 offset:0\n\t"
      "s_waitcnt lgkmcnt(0)\n\t"
      "v_mfma_f32_16x16x32_bf16 v[42:45], v[154:157], v[122:125], v[42:45]\n\t"
      "ds_read_b32 v126, v190 offset:0\n\t"
      "ds_read_b32 v127, v191 offset:0\n\t"
      "ds_read_b32 v128, v192 offset:0\n\t"
      "ds_read_b32 v129, v193 offset:0\n\t"
      "s_waitcnt lgkmcnt(0)\n\t"
      "v_mfma_f32_16x16x32_bf16 v[46:49], v[158:161], v[126:129], v[46:49]\n\t"
      "ds_read_b128 v[98:101], v241 offset:3072\n\t"
      "ds_read_b32 v130, v194 offset:0\n\t"
      "ds_read_b32 v131, v195 offset:0\n\t"
      "ds_read_b32 v132, v196 offset:0\n\t"
      "ds_read_b32 v133, v197 offset:0\n\t"
      "s_waitcnt lgkmcnt(0)\n\t"
      "v_mfma_f32_16x16x32_bf16 v[50:53], v[162:165], v[130:133], v[50:53]\n\t"
      "ds_read_b32 v230, v206 offset:0\n\t"
      "global_load_dwordx4 a[192:195], v245, s[40:41] offset:3072 sc1\n\t"
      "ds_read_b32 v231, v207 offset:0\n\t"
      "s_waitcnt lgkmcnt(0)\n\t"
      "v_mfma_f32_16x16x16_bf16 v[38:41], v[198:199], v[230:231], v[38:41]\n\t"
      "ds_read_b32 v232, v208 offset:0\n\t"
      "ds_read_b32 v233, v209 offset:0\n\t"
      "s_waitcnt lgkmcnt(0)\n\t"
      "v_mfma_f32_16x16x16_bf16 v[42:45], v[200:201], v[232:233], v[42:45]\n\t"
      "ds_read_b32 v234, v210 offset:0\n\t"
      "ds_read_b32 v235, v211 offset:0\n\t"
      "s_waitcnt lgkmcnt(0)\n\t"
      "v_mfma_f32_16x16x16_bf16 v[46:49], v[202:203], v[234:235], v[46:49]\n\t"
      "ds_read_b32 v236, v212 offset:0\n\t"
      "s_add_u32 s40, s40, 4096\n\t"
      "s_addc_u32 s41, s41, 0\n\t"
      "global_load_dwordx4 a[196:199], v245, s[40:41] sc1\n\t"
      "ds_read_b32 v237, v213 offset:0\n\t"
      "s_waitcnt lgkmcnt(0)\n\t"
      "v_mfma_f32_16x16x16_bf16 v[50:53], v[204:205], v[236:237], v[50:53]\n\t"
      "s_cbranch_vccnz .Lgp0_%=\n\t"
      ".Lgq0_%=:\n\t"
      "v_smfmac_f32_16x16x64_bf16 v[54:57], a[96:99], v[102:109], v18\n\t"
      "ds_read_b32 v214, v166 offset:2048\n\t"
      "v_smfmac_f32_16x16x64_bf16 v[54:57], a[112:115], v[110:117], v20\n\t"
      "ds_read_b32 v215, v167 offset:2048\n\t"
      "global_load_dwordx4 a[200:203], v245, s[40:41] offset:1024 sc1\n\t"
      "v_smfmac_f32_16x16x64_bf16 v[58:61], a[100:103], v[102:109], v18 abid:1\n\t"
      "ds_read_b32 v216, v168 offset:2048\n\t"
      "v_smfmac_f32_16x16x64_bf16 v[58:61], a[116:119], v[110:117], v20 abid:1\n\t"
      "s_mov_b64 exec, s[68:69]\n\t"
      "ds_read_b32 v217, v169 offset:2048\n\t"
      "s_mov_b64 exec, -1\n\t"
      "v_smfmac_f32_16x16x64_bf16 v[62:65], a[104:107], v[102:109], v19\n\t"
      "ds_read_b32 v218, v170 offset:2048\n\t"
      "global_load_dwordx4 a[204:207], v245, s[40:41] offset:2048 sc1\n\t"
      "v_smfmac_f32_16x16x64_bf16 v[62:65], a[120:123], v[110:117], v21\n\t"
      "ds_read_b32 v219, v171 offset:2048\n\t"
      "v_smfmac_f32_16x16x64_bf16 v[66:69], a[108:111], v[102:109], v19 abid:1\n\t"
      "ds_read_b32 v220, v172 offset:2048\n\t"
      "v_smfmac_f32_16x16x64_bf16 v[66:69], a[124:127], v[110:117], v21 abid:1\n\t"
      "s_mov_b64 exec, s[68:69]\n\t"
      "ds_read_b32 v221, v173 offset:2048\n\t"
      "s_mov_b64 exec, -1\n\t"
      "global_load_dwordx4 a[208:211], v245, s[40:41] offset:3072 sc1\n\t"
      "s_waitcnt vmcnt(17)\n\t"
      "v_smfmac_f32_16x16x64_bf16 v[38:41], a[128:131], v[70:77], v22\n\t"
      "ds_read_b128 v[102:105], v238 offset:1280\n\t"
      "v_smfmac_f32_16x16x64_bf16 v[38:41], a[144:147], v[78:85], v24\n\t"
      "ds_read_b32 v222, v174 offset:2048\n\t"
      "v_smfmac_f32_16x16x64_bf16 v[42:45], a[132:135], v[70:77], v22 abid:1\n\t"
      "ds_read_b128 v[106:109], v239 offset:1280\n\t"
      "s_add_u32 s40, s40, 4096\n\t"
      "s_addc_u32 s41, s41, 0\n\t"
      "global_load_dwordx4 a[212:215], v245, s[40:41] sc1\n\t"
      "v_smfmac_f32_16x16x64_bf16 v[42:45], a[148:151], v[78:85], v24 abid:1\n\t"
      "ds_read_b32 v223, v175 offset:2048\n\t"
      "v_smfmac_f32_16x16x64_bf16 v[46:49], a[136:139], v[70:77], v23\n\t"
      "ds_read_b128 v[110:113], v240 offset:1280\n\t"
      "v_smfmac_f32_16x16x64_bf16 v[46:49], a[152:155], v[78:85], v25\n\t"
      "ds_read_b32 v224, v176 offset:2048\n\t"
      "global_load_dwordx4 a[216:219], v245, s[40:41] offset:1024 sc1\n\t"
      "v_smfmac_f32_16x16x64_bf16 v[50:53], a[140:143], v[70:77], v23 abid:1\n\t"
      "s_waitcnt lgkmcnt(8)\n\t"
      "ds_read_b128 v[114:117], v241 offset:1280\n\t"
      "v_smfmac_f32_16x16x64_bf16 v[50:53], a[156:159], v[78:85], v25 abid:1\n\t"
      "s_mov_b64 exec, s[68:69]\n\t"
      "ds_read_b32 v225, v177 offset:2048\n\t"
      "s_mov_b64 exec, -1\n\t"
      "v_smfmac_f32_16x16x64_bf16 v[54:57], a[128:131], v[86:93], v22\n\t"
      "ds_read_b128 v[70:73], v238 offset:3328\n\t"
      "global_load_dwordx4 a[220:223], v245, s[40:41] offset:2048 sc1\n\t"
      "v_smfmac_f32_16x16x64_bf16 v[54:57], a[144:147], v[94:101], v24\n\t"
      "ds_read_b32 v226, v178 offset:2048\n\t"
      "v_smfmac_f32_16x16x64_bf16 v[58:61], a[132:135], v[86:93], v22 abid:1\n\t"
      "ds_read_b128 v[74:77], v239 offset:3328\n\t"
      "v_smfmac_f32_16x16x64_bf16 v[58:61], a[148:151], v[94:101], v24 abid:1\n\t"
      "ds_read_b32 v227, v179 offset:2048\n\t"
      "global_load_dwordx2 v[34:35], v252, s[40:41] offset:3072 sc1\n\t"
      "global_load_dwordx2 v[36:37], v252, s[40:41] offset:3584 sc1\n\t"
      "v_smfmac_f32_16x16x64_bf16 v[62:65], a[136:139], v[86:93], v23\n\t"
      "s_waitcnt lgkmcnt(8)\n\t"
      "ds_read_b128 v[78:81], v240 offset:3328\n\t"
      "v_smfmac_f32_16x16x64_bf16 v[62:65], a[152:155], v[94:101], v25\n\t"
      "ds_read_b32 v228, v180 offset:2048\n\t"
      "v_smfmac_f32_16x16x64_bf16 v[66:69], a[140:143], v[86:93], v23 abid:1\n\t"
      "ds_read_b128 v[82:85], v241 offset:3328\n\t"
      "s_add_u32 s40, s40, 4096\n\t"
      "s_addc_u32 s41, s41, 0\n\t"
      "global_load_dwordx4 a[224:227], v245, s[40:41] sc1\n\t"
      "v_smfmac_f32_16x16x64_bf16 v[66:69], a[156:159], v[94:101], v25 abid:1\n\t"
      "s_mov_b64 exec, s[68:69]\n\t"
      "ds_read_b32 v229, v181 offset:2048\n\t"
      "s_mov_b64 exec, -1\n\t"
      "s_waitcnt vmcnt(13)\n\t"
      "s_waitcnt lgkmcnt(9)\n\t"
      "v_smfmac_f32_16x16x64_bf16 v[38:41], a[160:163], v[102:109], v26\n\t"
      "ds_read_b128 v[86:89], v238 offset:1536\n\t"
      "v_smfmac_f32_16x16x64_bf16 v[38:41], a[176:179], v[110:117], v28\n\t"
      "ds_read_b32 v118, v182 offset:2048\n\t"
      "global_load_dwordx4 a[228:231], v245, s[40:41] offset:1024 sc1\n\t"
      "v_smfmac_f32_16x16x64_bf16 v[42:45], a[164:167], v[102:109], v26 abid:1\n\t"
      "ds_read_b128 v[90:93], v239 offset:1536\n\t"
      "v_smfmac_f32_16x16x64_bf16 v[42:45], a[180:183], v[110:117], v28 abid:1\n\t"
      "ds_read_b32 v119, v183 offset:2048\n\t"
      "v_smfmac_f32_16x16x64_bf16 v[46:49], a[168:171], v[102:109], v27\n\t"
      "ds_read_b128 v[94:97], v240 offset:1536\n\t"
      "global_load_dwordx4 a[232:235], v245, s[40:41] offset:2048 sc1\n\t"
      "v_smfmac_f32_16x16x64_bf16 v[46:49], a[184:187], v[110:117], v29\n\t"
      "s_waitcnt lgkmcnt(8)\n\t"
      "ds_read_b32 v120, v184 offset:2048\n\t"
      "v_smfmac_f32_16x16x64_bf16 v[50:53], a[172:175], v[102:109], v27 abid:1\n\t"
      "ds_read_b128 v[98:101], v241 offset:1536\n\t"
      "v_smfmac_f32_16x16x64_bf16 v[50:53], a[188:191], v[110:117], v29 abid:1\n\t"
      "ds_read_b32 v121, v185 offset:2048\n\t"
      "global_load_dwordx4 a[236:239], v245, s[40:41] offset:3072 sc1\n\t"
      "s_waitcnt lgkmcnt(9)\n\t"
      "v_mfma_f32_16x16x32_bf16 v[54:57], v[134:137], v[214:217], v[54:57]\n\t"
      "ds_read_b128 v[102:105], v238 offset:3584\n\t"
      "v_mfma_f32_16x16x32_bf16 v[58:61], v[138:141], v[218:221], v[58:61]\n\t"
      "ds_read_b32 v122, v186 offset:2048\n\t"
      "v_mfma_f32_16x16x32_bf16 v[62:65], v[142:145], v[222:225], v[62:65]\n\t"
      "ds_read_b128 v[106:109], v239 offset:3584\n\t"
      "s_add_u32 s40, s40, 4096\n\t"
      "s_addc_u32 s41, s41, 0\n\t"
      "global_load_dwordx4 a[240:243], v245, s[40:41] sc1\n\t"
      "s_waitcnt lgkmcnt(11)\n\t"
      "v_mfma_f32_16x16x32_bf16 v[66:69], v[146:149], v[226:229], v[66:69]\n\t"
      "ds_read_b32 v123, v187 offset:2048\n\t"
      "s_waitcnt lgkmcnt(4)\n\t"
      "v_mfma_f32_16x16x32_bf16 v[54:57], v[150:153], v[118:121], v[54:57]\n\t"
      "ds_read_b128 v[110:113], v240 offset:3584\n\t"
      "ds_read_b32 v124, v188 offset:2048\n\t"
      "ds_read_b32 v125, v189 offset:2048\n\t"
      "s_waitcnt lgkmcnt(0)\n\t"
      "v_mfma_f32_16x16x32_bf16 v[58:61], v[154:157], v[122:125], v[58:61]\n\t"
      "ds_read_b32 v126, v190 offset:2048\n\t"
      "global_load_dwordx4 a[244:247], v245, s[40:41] offset:1024 sc1\n\t"
      "ds_read_b32 v127, v191 offset:2048\n\t"
      "ds_read_b32 v128, v192 offset:2048\n\t"
      "ds_read_b32 v129, v193 offset:2048\n\t"
      "s_waitcnt lgkmcnt(0)\n\t"
      "v_mfma_f32_16x16x32_bf16 v[62:65], v[158:161], v[126:129], v[62:65]\n\t"
      "ds_read_b128 v[114:117], v241 offset:3584\n\t"
      "ds_read_b32 v130, v194 offset:2048\n\t"
      "ds_read_b32 v131, v195 offset:2048\n\t"
      "ds_read_b32 v132, v196 offset:2048\n\t"
      "ds_read_b32 v133, v197 offset:2048\n\t"
      "s_waitcnt lgkmcnt(0)\n\t"
      "v_mfma_f32_16x16x32_bf16 v[66:69], v[162:165], v[130:133], v[66:69]\n\t"
      "ds_read_b32 v230, v206 offset:2048\n\t"
      "ds_read_b32 v231, v207 offset:2048\n\t"
      "s_waitcnt lgkmcnt(0)\n\t"
      "v_mfma_f32_16x16x16_bf16 v[54:57], v[198:199], v[230:231], v[54:57]\n\t"
      "ds_read_b32 v232, v208 offset:2048\n\t"
      "global_load_dwordx4 a[248:251], v245, s[40:41] offset:2048 sc1\n\t"
      "ds_read_b32 v233, v209 offset:2048\n\t"
      "s_waitcnt lgkmcnt(0)\n\t"
      "v_mfma_f32_16x16x16_bf16 v[58:61], v[200:201], v[232:233], v[58:61]\n\t"
      "ds_read_b32 v234, v210 offset:2048\n\t"
      "ds_read_b32 v235, v211 offset:2048\n\t"
      "s_waitcnt lgkmcnt(0)\n\t"
      "v_mfma_f32_16x16x16_bf16 v[62:65], v[202:203], v[234:235], v[62:65]\n\t"
      "ds_read_b32 v236, v212 offset:2048\n\t"
      "ds_read_b32 v237, v213 offset:2048\n\t"
      "s_waitcnt lgkmcnt(0)\n\t"
      "v_mfma_f32_16x16x16_bf16 v[66:69], v[204:205], v[236:237], v[66:69]\n\t"
      "global_load_dwordx4 a[252:255], v245, s[40:41] offset:3072 sc1\n\t"
      "s_cbranch_vccnz .Lgp1_%=\n\t"
      ".Lgq1_%=:\n\t"
      "v_xor_b32 v166, s57, v166\n\t"
      "v_xor_b32 v167, s57, v167\n\t"
      "v_xor_b32 v168, s57, v168\n\t"
      "v_xor_b32 v169, s57, v169\n\t"
      "v_xor_b32 v170, s57, v170\n\t"
      "v_xor_b32 v171, s57, v171\n\t"
      "v_xor_b32 v172, s57, v172\n\t"
      "v_xor_b32 v173, s57, v173\n\t"
      "v_xor_b32 v174, s57, v174\n\t"
      "v_xor_b32 v175, s57, v175\n\t"
      "v_xor_b32 v176, s57, v176\n\t"
      "v_xor_b32 v177, s57, v177\n\t"
      "v_xor_b32 v178, s57, v178\n\t"
      "v_xor_b32 v179, s57, v179\n\t"
      "v_xor_b32 v180, s57, v180\n\t"
      "v_xor_b32 v181, s57, v181\n\t"
      "v_smfmac_f32_16x16x64_bf16 v[54:57], a[160:163], v[70:77], v26\n\t"
      "ds_read_b32 v214, v166 offset:0\n\t"
      "v_xor_b32 v182, s57, v182\n\t"
      "v_smfmac_f32_16x16x64_bf16 v[54:57], a[176:179], v[78:85], v28\n\t"
      "ds_read_b32 v215, v167 offset:0\n\t"
      "v_xor_b32 v183, s57, v183\n\t"
      "v_smfmac_f32_16x16x64_bf16 v[58:61], a[164:167], v[70:77], v26 abid:1\n\t"
      "ds_read_b32 v216, v168 offset:0\n\t"
      "v_xor_b32 v184, s57, v184\n\t"
      "v_smfmac_f32_16x16x64_bf16 v[58:61], a[180:183], v[78:85], v28 abid:1\n\t"
      "s_mov_b64 exec, s[68:69]\n\t"
      "ds_read_b32 v217, v169 offset:0\n\t"
      "s_mov_b64 exec, -1\n\t"
      "v_xor_b32 v185, s57, v185\n\t"
      "v_smfmac_f32_16x16x64_bf16 v[62:65], a[168:171], v[70:77], v27\n\t"
      "ds_read_b32 v218, v170 offset:0\n\t"
      "v_xor_b32 v186, s57, v186\n\t"
      "v_smfmac_f32_16x16x64_bf16 v[62:65], a[184:187], v[78:85], v29\n\t"
      "ds_read_b32 v219, v171 offset:0\n\t"
      "v_xor_b32 v187, s57, v187\n\t"
      "v_smfmac_f32_16x16x64_bf16 v[66:69], a[172:175], v[70:77], v27 abid:1\n\t"
      "ds_read_b32 v220, v172 offset:0\n\t"
      "v_xor_b32 v188, s57, v188\n\t"
      "v_smfmac_f32_16x16x64_bf16 v[66:69], a[188:191], v[78:85], v29 abid:1\n\t"
      "s_mov_b64 exec, s[68:69]\n\t"
      "ds_read_b32 v221, v173 offset:0\n\t"
      "s_mov_b64 exec, -1\n\t"
      "v_xor_b32 v189, s57, v189\n\t"
      "s_waitcnt vmcnt(10)\n\t"
      "v_smfmac_f32_16x16x64_bf16 v[38:41], a[192:195], v[86:93], v30\n\t"
      "ds_read_b128 v[70:73], v238 offset:1792\n\t"
      "v_xor_b32 v190, s57, v190\n\t"
      "v_smfmac_f32_16x16x64_bf16 v[38:41], a[208:211], v[94:101], v32\n\t"
      "ds_read_b32 v222, v174 offset:0\n\t"
      "v_xor_b32 v191, s57, v191\n\t"
      "v_smfmac_f32_16x16x64_bf16 v[42:45], a[196:199], v[86:93], v30 abid:1\n\t"
      "ds_read_b128 v[74:77], v239 offset:1792\n\t"
      "v_xor_b32 v192, s57, v192\n\t"
      "v_smfmac_f32_16x16x64_bf16 v[42:45], a[212:215], v[94:101], v32 abid:1\n\t"
      "ds_read_b32 v223, v175 offset:0\n\t"
      "v_xor_b32 v193, s57, v193\n\t"
      "v_smfmac_f32_16x16x64_bf16 v[46:49], a[200:203], v[86:93], v31\n\t"
      "ds_read_b128 v[78:81], v240 offset:1792\n\t"
      "v_xor_b32 v194, s57, v194\n\t"
      "v_smfmac_f32_16x16x64_bf16 v[46:49], a[216:219], v[94:101], v33\n\t"
      "ds_read_b32 v224, v176 offset:0\n\t"
      "v_xor_b32 v195, s57, v195\n\t"
      "v_smfmac_f32_16x16x64_bf16 v[50:53], a[204:207], v[86:93], v31 abid:1\n\t"
      "s_waitcnt lgkmcnt(8)\n\t"
      "ds_read_b128 v[82:85], v241 offset:1792\n\t"
      "v_xor_b32 v196, s57, v196\n\t"
      "v_smfmac_f32_16x16x64_bf16 v[50:53], a[220:223], v[94:101], v33 abid:1\n\t"
      "s_mov_b64 exec, s[68:69]\n\t"
      "ds_read_b32 v225, v177 offset:0\n\t"
      "s_mov_b64 exec, -1\n\t"
      "v_xor_b32 v197, s57, v197\n\t"
      "v_smfmac_f32_16x16x64_bf16 v[54:57], a[192:195], v[102:109], v30\n\t"
      "ds_read_b128 v[86:89], v238 offset:3840\n\t"
      "v_xor_b32 v206, s57, v206\n\t"
      "v_smfmac_f32_16x16x64_bf16 v[54:57], a[208:211], v[110:117], v32\n\t"
      "ds_read_b32 v226, v178 offset:0\n\t"
      "v_xor_b32 v207, s57, v207\n\t"
      "v_smfmac_f32_16x16x64_bf16 v[58:61], a[196:199], v[102:109], v30 abid:1\n\t"
      "ds_read_b128 v[90:93], v239 offset:3840\n\t"
      "v_xor_b32 v208, s57, v208\n\t"
      "v_smfmac_f32_16x16x64_bf16 v[58:61], a[212:215], v[110:117], v32 abid:1\n\t"
      "ds_read_b32 v227, v179 offset:0\n\t"
      "v_xor_b32 v209, s57, v209\n\t"
      "v_smfmac_f32_16x16x64_bf16 v[62:65], a[200:203], v[102:109], v31\n\t"
      "s_waitcnt lgkmcnt(8)\n\t"
      "ds_read_b128 v[94:97], v240 offset:3840\n\t"
      "v_xor_b32 v210, s57, v210\n\t"
      "v_smfmac_f32_16x16x64_bf16 v[62:65], a[216:219], v[110:117], v33\n\t"
      "ds_read_b32 v228, v180 offset:0\n\t"
      "v_xor_b32 v211, s57, v211\n\t"
      "v_smfmac_f32_16x16x64_bf16 v[66:69], a[204:207], v[102:109], v31 abid:1\n\t"
      "ds_read_b128 v[98:101], v241 offset:3840\n\t"
      "v_xor_b32 v212, s57, v212\n\t"
      "v_smfmac_f32_16x16x64_bf16 v[66:69], a[220:223], v[110:117], v33 abid:1\n\t"
      "s_mov_b64 exec, s[68:69]\n\t"
      "ds_read_b32 v229, v181 offset:0\n\t"
      "s_mov_b64 exec, -1\n\t"
      "v_xor_b32 v213, s57, v213\n\t"
      "s_waitcnt vmcnt(0)\n\t"
      "s_waitcnt lgkmcnt(9)\n\t"
      "v_smfmac_f32_16x16x64_bf16 v[38:41], a[224:227], v[70:77], v34\n\t"
      "v_smfmac_f32_16x16x64_bf16 v[38:41], a[240:243], v[78:85], v36\n\t"
      "v_smfmac_f32_16x16x64_bf16 v[42:45], a[228:231], v[70:77], v34 abid:1\n\t"
      "v_smfmac_f32_16x16x64_bf16 v[42:45], a[244:247], v[78:85], v36 abid:1\n\t"
      "v_smfmac_f32_16x16x64_bf16 v[46:49], a[232:235], v[70:77], v35\n\t"
      "v_smfmac_f32_16x16x64_bf16 v[46:49], a[248:251], v[78:85], v37\n\t"
      "v_smfmac_f32_16x16x64_bf16 v[50:53], a[236:239], v[70:77], v35 abid:1\n\t"
      "v_smfmac_f32_16x16x64_bf16 v[50:53], a[252:255], v[78:85], v37 abid:1\n\t"
      "v_and_b32 v249, 7, %0\n\t"
      "v_bfe_u32 v250, %0, 3, 1\n\t"
      "v_lshlrev_b32 v244, 4, v248\n\t"
      "v_lshl_add_u32 v244, v250, 6, v244\n\t"
      "v_lshl_add_u32 v244, v249, 11, v244\n\t"
      "v_add_u32 v252, 16384, v244\n\t"
      "v_and_b32 v249, 12, v247\n\t"
      "global_load_dword v242, v249, %8 offset:2048 sc1\n\t"
      "s_barrier\n\t"
      "v_xor_b32 v238, s57, v238\n\t"
      "v_xor_b32 v239, s57, v239\n\t"
      "v_xor_b32 v240, s57, v240\n\t"
      "v_xor_b32 v241, s57, v241\n\t"
      "s_waitcnt lgkmcnt(1)\n\t"
      "v_smfmac_f32_16x16x64_bf16 v[54:57], a[224:227], v[86:93], v34\n\t"
      "ds_read_b128 v[70:73], v238 offset:0\n\t"
      "v_cvt_pk_bf16_f32 v38, v38, v39\n\t"
      "s_add_u32 m0, s46, 0\n\t"
      "s_nop 0\n\t"
      "global_load_lds_dwordx4 %11, s[36:37]\n\t"
      "v_smfmac_f32_16x16x64_bf16 v[54:57], a[240:243], v[94:101], v36\n\t"
      "ds_read_b128 v[74:77], v239 offset:0\n\t"
      "v_cvt_pk_bf16_f32 v39, v40, v41\n\t"
      "global_load_lds_dwordx4 %11, s[36:37] offset:1024\n\t"
      "v_smfmac_f32_16x16x64_bf16 v[58:61], a[228:231], v[86:93], v34 abid:1\n\t"
      "ds_read_b128 v[78:81], v240 offset:0\n\t"
      "v_cvt_pk_bf16_f32 v40, v42, v43\n\t"
      "s_add_u32 m0, s47, 0\n\t"
      "s_nop 0\n\t"
      "global_load_lds_dwordx4 %12, s[36:37]\n\t"
      "v_smfmac_f32_16x16x64_bf16 v[58:61], a[244:247], v[94:101], v36 abid:1\n\t"
      "ds_read_b128 v[82:85], v241 offset:0\n\t"
      "v_cvt_pk_bf16_f32 v41, v44, v45\n\t"
      "global_load_lds_dwordx4 %12, s[36:37] offset:1024\n\t"
      "v_smfmac_f32_16x16x64_bf16 v[62:65], a[232:235], v[86:93], v35\n\t"
      "v_cvt_pk_bf16_f32 v46, v46, v47\n\t"
      "s_add_u32 m0, s48, 0\n\t"
      "s_nop 0\n\t"
      "global_load_lds_dwordx4 %13, s[36:37]\n\t"
      "v_smfmac_f32_16x16x64_bf16 v[62:65], a[248:251], v[94:101], v37\n\t"
      "v_cvt_pk_bf16_f32 v47, v48, v49\n\t"
      "global_load_lds_dwordx4 %13, s[36:37] offset:1024\n\t"
      "v_smfmac_f32_16x16x64_bf16 v[66:69], a[236:239], v[86:93], v35 abid:1\n\t"
      "v_cvt_pk_bf16_f32 v48, v50, v51\n\t"
      "s_add_u32 m0, s49, 0\n\t"
      "s_nop 0\n\t"
      "global_load_lds_dwordx4 %14, s[36:37]\n\t"
      "v_smfmac_f32_16x16x64_bf16 v[66:69], a[252:255], v[94:101], v37 abid:1\n\t"
      "v_cvt_pk_bf16_f32 v49, v52, v53\n\t"
      "global_load_lds_dwordx4 %14, s[36:37] offset:1024\n\t"
      "s_add_u32 s36, s36, 2097152\n\t"
      "s_addc_u32 s37, s37, 0\n\t"
      "s_nop 1\n\t"
      "v_mov_b32_dpp v42, v46 row_ror:8 row_mask:0xf bank_mask:0xf\n\t"
      "v_mov_b32_dpp v43, v47 row_ror:8 row_mask:0xf bank_mask:0xf\n\t"
      "v_mov_b32_dpp v44, v48 row_ror:8 row_mask:0xf bank_mask:0xf\n\t"
      "v_mov_b32_dpp v45, v49 row_ror:8 row_mask:0xf bank_mask:0xf\n\t"
      "v_mov_b32_dpp v46, v38 row_ror:8 row_mask:0xf bank_mask:0x3\n\t"
      "v_mov_b32_dpp v47, v39 row_ror:8 row_mask:0xf bank_mask:0x3\n\t"
      "v_mov_b32_dpp v48, v40 row_ror:8 row_mask:0xf bank_mask:0x3\n\t"
      "v_mov_b32_dpp v49, v41 row_ror:8 row_mask:0xf bank_mask:0x3\n\t"
      "v_mov_b32_dpp v38, v42 quad_perm:[0,1,2,3] row_mask:0xf bank_mask:0xc\n\t"
      "v_mov_b32_dpp v39, v43 quad_perm:[0,1,2,3] row_mask:0xf bank_mask:0xc\n\t"
      "v_mov_b32_dpp v40, v44 quad_perm:[0,1,2,3] row_mask:0xf bank_mask:0xc\n\t"
      "v_mov_b32_dpp v41, v45 quad_perm:[0,1,2,3] row_mask:0xf bank_mask:0xc\n\t"
      "global_store_dwordx4 v244, v[38:41], s[38:39] sc1\n\t"
      "global_store_dwordx4 v252, v[46:49], s[38:39] sc1\n\t"
      "s_add_u32 s38, s38, 2097152\n\t"
      "s_addc_u32 s39, s39, 0\n\t"
      "s_waitcnt lgkmcnt(0)\n\t"
      ".LloopB%=:\n\t"
      "s_waitcnt lgkmcnt(5)\n\t"
      "v_mfma_f32_16x16x32_bf16 v[38:41], v[134:137], v[214:217], 0\n\t"
      "ds_read_b128 v[86:89], v238 offset:256\n\t"
      "v_mfma_f32_16x16x32_bf16 v[42:45], v[138:141], v[218:221], 0\n\t"
      "ds_read_b32 v118, v182 offset:0\n\t"
      "s_waitcnt lgkmcnt(6)\n\t"
      "v_mfma_f32_16x16x32_bf16 v[46:49], v[142:145], v[222:225], 0\n\t"
      "ds_read_b128 v[90:93], v239 offset:256\n\t"
      "s_waitcnt lgkmcnt(3)\n\t"
      "v_mfma_f32_16x16x32_bf16 v[50:53], v[146:149], v[226:229], 0\n\t"
      "ds_read_b32 v119, v183 offset:0\n\t"
      "v_smfmac_f32_16x16x64_bf16 v[38:41], a[0:3], v[70:77], v6\n\t"
      "ds_read_b128 v[94:97], v240 offset:256\n\t"
      "v_smfmac_f32_16x16x64_bf16 v[38:41], a[16:19], v[78:85], v8\n\t"
      "ds_read_b32 v120, v184 offset:0\n\t"
      "v_smfmac_f32_16x16x64_bf16 v[42:45], a[4:7], v[70:77], v6 abid:1\n\t"
      "ds_read_b128 v[98:101], v241 offset:256\n\t"
      "v_smfmac_f32_16x16x64_bf16 v[42:45], a[20:23], v[78:85], v8 abid:1\n\t"
      "ds_read_b32 v121, v185 offset:0\n\t"
      "v_smfmac_f32_16x16x64_bf16 v[46:49], a[8:11], v[70:77], v7\n\t"
      "ds_read_b128 v[102:105], v238 offset:512\n\t"
      "v_smfmac_f32_16x16x64_bf16 v[46:49], a[24:27], v[78:85], v9\n\t"
      "ds_read_b32 v122, v186 offset:0\n\t"
      "v_smfmac_f32_16x16x64_bf16 v[50:53], a[12:15], v[70:77], v7 abid:1\n\t"
      "ds_read_b128 v[106:109], v239 offset:512\n\t"
      "v_smfmac_f32_16x16x64_bf16 v[50:53], a[28:31], v[78:85], v9 abid:1\n\t"
      "ds_read_b32 v123, v187 offset:0\n\t"
      "s_waitcnt lgkmcnt(5)\n\t"
      "v_smfmac_f32_16x16x64_bf16 v[38:41], a[32:35], v[86:93], v10\n\t"
      "ds_read_b128 v[110:113], v240 offset:512\n\t"
      "v_cvt_pk_bf16_f32 v54, v54, v55\n\t"
      "v_smfmac_f32_16x16x64_bf16 v[38:41], a[48:51], v[94:101], v12\n\t"
      "ds_read_b32 v124, v188 offset:0\n\t"
      "v_cvt_pk_bf16_f32 v55, v56, v57\n\t"
      "v_smfmac_f32_16x16x64_bf16 v[42:45], a[36:39], v[86:93], v10 abid:1\n\t"
      "ds_read_b128 v[114:117], v241 offset:512\n\t"
      "v_cvt_pk_bf16_f32 v56, v58, v59\n\t"
      "v_smfmac_f32_16x16x64_bf16 v[42:45], a[52:55], v[94:101], v12 abid:1\n\t"
      "ds_read_b32 v125, v189 offset:0\n\t"
      "v_cvt_pk_bf16_f32 v57, v60, v61\n\t"
      "v_smfmac_f32_16x16x64_bf16 v[46:49], a[40:43], v[86:93], v11\n\t"
      "ds_read_b128 v[70:73], v238 offset:768\n\t"
      "v_cvt_pk_bf16_f32 v62, v62, v63\n\t"
      "v_smfmac_f32_16x16x64_bf16 v[46:49], a[56:59], v[94:101], v13\n\t"
      "ds_read_b32 v126, v190 offset:0\n\t"
      "v_cvt_pk_bf16_f32 v63, v64, v65\n\t"
      "v_smfmac_f32_16x16x64_bf16 v[50:53], a[44:47], v[86:93], v11 abid:1\n\t"
      "ds_read_b128 v[74:77], v239 offset:768\n\t"
      "v_cvt_pk_bf16_f32 v64, v66, v67\n\t"
      "v_smfmac_f32_16x16x64_bf16 v[50:53], a[60:63], v[94:101], v13 abid:1\n\t"
      "ds_read_b32 v127, v191 offset:0\n\t"
      "v_cvt_pk_bf16_f32 v65, v68, v69\n\t"
      "s_waitcnt lgkmcnt(5)\n\t"
      "v_smfmac_f32_16x16x64_bf16 v[38:41], a[64:67], v[102:109], v14\n\t"
      "ds_read_b128 v[78:81], v240 offset:768\n\t"
      "v_mov_b32_dpp v58, v62 row_ror:8 row_mask:0xf bank_mask:0xf\n\t"
      "v_smfmac_f32_16x16x64_bf16 v[38:41], a[80:83], v[110:117], v16\n\t"
      "ds_read_b32 v128, v192 offset:0\n\t"
      "v_mov_b32_dpp v59, v63 row_ror:8 row_mask:0xf bank_mask:0xf\n\t"
      "v_smfmac_f32_16x16x64_bf16 v[42:45], a[68:71], v[102:109], v14 abid:1\n\t"
      "ds_read_b128 v[82:85], v241 offset:768\n\t"
      "v_mov_b32_dpp v60, v64 row_ror:8 row_mask:0xf bank_mask:0xf\n\t"
      "v_smfmac_f32_16x16x64_bf16 v[42:45], a[84:87], v[110:117], v16 abid:1\n\t"
      "ds_read_b32 v129, v193 offset:0\n\t"
      "v_mov_b32_dpp v61, v65 row_ror:8 row_mask:0xf bank_mask:0xf\n\t"
      "v_smfmac_f32_16x16x64_bf16 v[46:49], a[72:75], v[102:109], v15\n\t"
      "ds_read_b128 v[86:89], v238 offset:1024\n\t"
      "v_mov_b32_dpp v62, v54 row_ror:8 row_mask:0xf bank_mask:0x3\n\t"
      "v_smfmac_f32_16x16x64_bf16 v[46:49], a[88:91], v[110:117], v17\n\t"
      "ds_read_b32 v130, v194 offset:0\n\t"
      "v_mov_b32_dpp v63, v55 row_ror:8 row_mask:0xf bank_mask:0x3\n\t"
      "v_smfmac_f32_16x16x64_bf16 v[50:53], a[76:79], v[102:109], v15 abid:1\n\t"
      "ds_read_b128 v[90:93], v239 offset:1024\n\t"
      "v_mov_b32_dpp v64, v56 row_ror:8 row_mask:0xf bank_mask:0x3\n\t"
      "v_smfmac_f32_16x16x64_bf16 v[50:53], a[92:95], v[110:117], v17 abid:1\n\t"
      "ds_read_b32 v131, v195 offset:0\n\t"
      "v_mov_b32_dpp v65, v57 row_ror:8 row_mask:0xf bank_mask:0x3\n\t"
      "s_waitcnt vmcnt(12)\n\t"
      "s_barrier\n\t"
      "s_xor_b32 s71, s67, 6144\n\t"
      "s_waitcnt lgkmcnt(5)\n\t"
      "v_smfmac_f32_16x16x64_bf16 v[38:41], a[96:99], v[70:77], v18\n\t"
      "ds_read_b128 v[94:97], v240 offset:1024\n\t"
      "v_mov_b32_dpp v54, v58 quad_perm:[0,1,2,3] row_mask:0xf bank_mask:0xc\n\t"
      "s_add_u32 m0, s46, s71\n\t"
      "s_nop 0\n\t"
      "global_load_lds_dwordx4 %11, s[36:37]\n\t"
      "v_smfmac_f32_16x16x64_bf16 v[38:41], a[112:115], v[78:85], v20\n\t"
      "ds_read_b32 v132, v196 offset:0\n\t"
      "v_mov_b32_dpp v55, v59 quad_perm:[0,1,2,3] row_mask:0xf bank_mask:0xc\n\t"
      "v_smfmac_f32_16x16x64_bf16 v[42:45], a[100:103], v[70:77], v18 abid:1\n\t"
      "ds_read_b128 v[98:101], v241 offset:1024\n\t"
      "v_mov_b32_dpp v56, v60 quad_perm:[0,1,2,3] row_mask:0xf bank_mask:0xc\n\t"
      "v_smfmac_f32_16x16x64_bf16 v[42:45], a[116:119], v[78:85], v20 abid:1\n\t"
      "ds_read_b32 v133, v197 offset:0\n\t"
      "v_mov_b32_dpp v57, v61 quad_perm:[0,1,2,3] row_mask:0xf bank_mask:0xc\n\t"
      "global_load_lds_dwordx4 %11, s[36:37] offset:1024\n\t"
      "v_smfmac_f32_16x16x64_bf16 v[46:49], a[104:107], v[70:77], v19\n\t"
      "ds_read_b128 v[102:105], v238 offset:1280\n\t"
      "v_smfmac_f32_16x16x64_bf16 v[46:49], a[120:123], v[78:85], v21\n\t"
      "ds_read_b128 v[106:109], v239 offset:1280\n\t"
      "v_smfmac_f32_16x16x64_bf16 v[50:53], a[108:111], v[70:77], v19 abid:1\n\t"
      "ds_read_b128 v[110:113], v240 offset:1280\n\t"
      "s_add_u32 m0, s47, s71\n\t"
      "s_nop 0\n\t"
      "global_load_lds_dwordx4 %12, s[36:37]\n\t"
      "v_smfmac_f32_16x16x64_bf16 v[50:53], a[124:127], v[78:85], v21 abid:1\n\t"
      "ds_read_b128 v[114:117], v241 offset:1280\n\t"
      "s_waitcnt lgkmcnt(5)\n\t"
      "v_mfma_f32_16x16x32_bf16 v[38:41], v[150:153], v[118:121], v[38:41]\n\t"
      "ds_read_b128 v[70:73], v238 offset:1536\n\t"
      "v_mfma_f32_16x16x32_bf16 v[42:45], v[154:157], v[122:125], v[42:45]\n\t"
      "ds_read_b32 v230, v206 offset:0\n\t"
      "global_load_lds_dwordx4 %12, s[36:37] offset:1024\n\t"
      "v_mfma_f32_16x16x32_bf16 v[46:49], v[158:161], v[126:129], v[46:49]\n\t"
      "ds_read_b128 v[74:77], v239 offset:1536\n\t"
      "s_waitcnt lgkmcnt(7)\n\t"
      "v_mfma_f32_16x16x32_bf16 v[50:53], v[162:165], v[130:133], v[50:53]\n\t"
      "ds_read_b32 v231, v207 offset:0\n\t"
      "v_smfmac_f32_16x16x64_bf16 v[38:41], a[128:131], v[86:93], v22\n\t"
      "ds_read_b128 v[78:81], v240 offset:1536\n\t"
      "s_add_u32 m0, s48, s71\n\t"
      "s_nop 0\n\t"
      "global_load_lds_dwordx4 %13, s[36:37]\n\t"
      "v_smfmac_f32_16x16x64_bf16 v[38:41], a[144:147], v[94:101], v24\n\t"
      "ds_read_b32 v232, v208 offset:0\n\t"
      "v_smfmac_f32_16x16x64_bf16 v[42:45], a[132:135], v[86:93], v22 abid:1\n\t"
      "ds_read_b128 v[82:85], v241 offset:1536\n\t"
      "v_smfmac_f32_16x16x64_bf16 v[42:45], a[148:151], v[94:101], v24 abid:1\n\t"
      "ds_read_b32 v233, v209 offset:0\n\t"
      "global_load_lds_dwordx4 %13, s[36:37] offset:1024\n\t"
      "v_smfmac_f32_16x16x64_bf16 v[46:49], a[136:139], v[86:93], v23\n\t"
      "ds_read_b32 v234, v210 offset:0\n\t"
      "v_smfmac_f32_16x16x64_bf16 v[46:49], a[152:155], v[94:101], v25\n\t"
      "ds_read_b32 v235, v211 offset:0\n\t"
      "v_smfmac_f32_16x16x64_bf16 v[50:53], a[140:143], v[86:93], v23 abid:1\n\t"
      "s_waitcnt lgkmcnt(8)\n\t"
      "ds_read_b32 v236, v212 offset:0\n\t"
      "s_add_u32 m0, s49, s71\n\t"
      "s_nop 0\n\t"
      "global_load_lds_dwordx4 %14, s[36:37]\n\t"
      "v_smfmac_f32_16x16x64_bf16 v[50:53], a[156:159], v[94:101], v25 abid:1\n\t"
      "ds_read_b32 v237, v213 offset:0\n\t"
      "v_smfmac_f32_16x16x64_bf16 v[38:41], a[160:163], v[102:109], v26\n\t"
      "ds_read_b128 v[86:89], v238 offset:1792\n\t"
      "v_smfmac_f32_16x16x64_bf16 v[38:41], a[176:179], v[110:117], v28\n\t"
      "ds_read_b32 v214, v166 offset:2048\n\t"
      "global_load_lds_dwordx4 %14, s[36:37] offset:1024\n\t"
      "s_add_u32 s36, s36, s73\n\t"
      "s_addc_u32 s37, s37, 0\n\t"
      "v_smfmac_f32_16x16x64_bf16 v[42:45], a[164:167], v[102:109], v26 abid:1\n\t"
      "ds_read_b128 v[90:93], v239 offset:1792\n\t"
      "v_smfmac_f32_16x16x64_bf16 v[42:45], a[180:183], v[110:117], v28 abid:1\n\t"
      "ds_read_b32 v215, v167 offset:2048\n\t"
      "v_smfmac_f32_16x16x64_bf16 v[46:49], a[168:171], v[102:109], v27\n\t"
      "s_waitcnt lgkmcnt(8)\n\t"
      "ds_read_b128 v[94:97], v240 offset:1792\n\t"
      "v_smfmac_f32_16x16x64_bf16 v[46:49], a[184:187], v[110:117], v29\n\t"
      "ds_read_b32 v216, v168 offset:2048\n\t"
      "v_smfmac_f32_16x16x64_bf16 v[50:53], a[172:175], v[102:109], v27 abid:1\n\t"
      "ds_read_b128 v[98:101], v241 offset:1792\n\t"
      "v_smfmac_f32_16x16x64_bf16 v[50:53], a[188:191], v[110:117], v29 abid:1\n\t"
      "s_mov_b64 exec, s[68:69]\n\t"
      "ds_read_b32 v217, v169 offset:2048\n\t"
      "s_mov_b64 exec, -1\n\t"
      "v_mfma_f32_16x16x16_bf16 v[38:41], v[198:199], v[230:231], v[38:41]\n\t"
      "ds_read_b128 v[102:105], v238 offset:2048\n\t"
      "v_mfma_f32_16x16x16_bf16 v[42:45], v[200:201], v[232:233], v[42:45]\n\t"
      "ds_read_b32 v218, v170 offset:2048\n\t"
      "s_waitcnt lgkmcnt(12)\n\t"
      "v_mfma_f32_16x16x16_bf16 v[46:49], v[202:203], v[234:235], v[46:49]\n\t"
      "ds_read_b128 v[106:109], v239 offset:2048\n\t"
      "s_waitcnt lgkmcnt(11)\n\t"
      "v_mfma_f32_16x16x16_bf16 v[50:53], v[204:205], v[236:237], v[50:53]\n\t"
      "ds_read_b32 v219, v171 offset:2048\n\t"
      "v_smfmac_f32_16x16x64_bf16 v[38:41], a[192:195], v[70:77], v30\n\t"
      "ds_read_b128 v[110:113], v240 offset:2048\n\t"
      "v_smfmac_f32_16x16x64_bf16 v[38:41], a[208:211], v[78:85], v32\n\t"
      "ds_read_b32 v220, v172 offset:2048\n\t"
      "v_smfmac_f32_16x16x64_bf16 v[42:45], a[196:199], v[70:77], v30 abid:1\n\t"
      "s_waitcnt lgkmcnt(8)\n\t"
      "ds_read_b128 v[114:117], v241 offset:2048\n\t"
      "v_smfmac_f32_16x16x64_bf16 v[42:45], a[212:215], v[78:85], v32 abid:1\n\t"
      "s_mov_b64 exec, s[68:69]\n\t"
      "ds_read_b32 v221, v173 offset:2048\n\t"
      "s_mov_b64 exec, -1\n\t"
      "v_smfmac_f32_16x16x64_bf16 v[46:49], a[200:203], v[70:77], v31\n\t"
      "ds_read_b32 v222, v174 offset:2048\n\t"
      "v_smfmac_f32_16x16x64_bf16 v[46:49], a[216:219], v[78:85], v33\n\t"
      "ds_read_b32 v223, v175 offset:2048\n\t"
      "v_smfmac_f32_16x16x64_bf16 v[50:53], a[204:207], v[70:77], v31 abid:1\n\t"
      "ds_read_b32 v224, v176 offset:2048\n\t"
      "v_smfmac_f32_16x16x64_bf16 v[50:53], a[220:223], v[78:85], v33 abid:1\n\t"
      "s_mov_b64 exec, s[68:69]\n\t"
      "ds_read_b32 v225, v177 offset:2048\n\t"
      "s_mov_b64 exec, -1\n\t"
      "s_cbranch_vccnz .Lgc0_%=\n\t"
      ".Lgr0_%=:\n\t"
      "s_waitcnt lgkmcnt(13)\n\t"
      "v_smfmac_f32_16x16x64_bf16 v[38:41], a[224:227], v[86:93], v34\n\t"
      "ds_read_b128 v[70:73], v238 offset:2304\n\t"
      "global_store_dwordx4 v244, v[54:57], s[38:39] sc1\n\t"
      "v_smfmac_f32_16x16x64_bf16 v[38:41], a[240:243], v[94:101], v36\n\t"
      "s_waitcnt lgkmcnt(8)\n\t"
      "ds_read_b32 v226, v178 offset:2048\n\t"
      "v_smfmac_f32_16x16x64_bf16 v[42:45], a[228:231], v[86:93], v34 abid:1\n\t"
      "ds_read_b128 v[74:77], v239 offset:2304\n\t"
      "global_store_dwordx4 v252, v[62:65], s[38:39] sc1\n\t"
      "v_smfmac_f32_16x16x64_bf16 v[42:45], a[244:247], v[94:101], v36 abid:1\n\t"
      "ds_read_b32 v227, v179 offset:2048\n\t"
      "v_smfmac_f32_16x16x64_bf16 v[46:49], a[232:235], v[86:93], v35\n\t"
      "ds_read_b128 v[78:81], v240 offset:2304\n\t"
      "s_add_u32 s38, s38, 2097152\n\t"
      "s_addc_u32 s39, s39, 0\n\t"
      "v_smfmac_f32_16x16x64_bf16 v[46:49], a[248:251], v[94:101], v37\n\t"
      "ds_read_b32 v228, v180 offset:2048\n\t"
      "v_smfmac_f32_16x16x64_bf16 v[50:53], a[236:239], v[86:93], v35 abid:1\n\t"
      "ds_read_b128 v[82:85], v241 offset:2304\n\t"
      "v_smfmac_f32_16x16x64_bf16 v[50:53], a[252:255], v[94:101], v37 abid:1\n\t"
      "s_mov_b64 exec, s[68:69]\n\t"
      "s_waitcnt lgkmcnt(8)\n\t"
      "ds_read_b32 v229, v181 offset:2048\n\t"
      "s_mov_b64 exec, -1\n\t"
      "v_mfma_f32_16x16x32_bf16 v[54:57], v[134:137], v[214:217], 0\n\t"
      "ds_read_b128 v[86:89], v238 offset:2560\n\t"
      "v_mfma_f32_16x16x32_bf16 v[58:61], v[138:141], v[218:221], 0\n\t"
      "ds_read_b32 v118, v182 offset:2048\n\t"
      "s_waitcnt lgkmcnt(10)\n\t"
      "v_mfma_f32_16x16x32_bf16 v[62:65], v[142:145], v[222:225], 0\n\t"
      "ds_read_b128 v[90:93], v239 offset:2560\n\t"
      "s_waitcnt lgkmcnt(3)\n\t"
      "v_mfma_f32_16x16x32_bf16 v[66:69], v[146:149], v[226:229], 0\n\t"
      "ds_read_b32 v119, v183 offset:2048\n\t"
      "v_smfmac_f32_16x16x64_bf16 v[54:57], a[0:3], v[102:109], v6\n\t"
      "ds_read_b128 v[94:97], v240 offset:2560\n\t"
      "v_smfmac_f32_16x16x64_bf16 v[54:57], a[16:19], v[110:117], v8\n\t"
      "ds_read_b32 v120, v184 offset:2048\n\t"
      "v_smfmac_f32_16x16x64_bf16 v[58:61], a[4:7], v[102:109], v6 abid:1\n\t"
      "ds_read_b128 v[98:101], v241 offset:2560\n\t"
      "v_smfmac_f32_16x16x64_bf16 v[58:61], a[20:23], v[110:117], v8 abid:1\n\t"
      "ds_read_b32 v121, v185 offset:2048\n\t"
      "v_smfmac_f32_16x16x64_bf16 v[62:65], a[8:11], v[102:109], v7\n\t"
      "ds_read_b32 v122, v186 offset:2048\n\t"
      "v_smfmac_f32_16x16x64_bf16 v[62:65], a[24:27], v[110:117], v9\n\t"
      "ds_read_b32 v123, v187 offset:2048\n\t"
      "v_smfmac_f32_16x16x64_bf16 v[66:69], a[12:15], v[102:109], v7 abid:1\n\t"
      "ds_read_b32 v124, v188 offset:2048\n\t"
      "v_smfmac_f32_16x16x64_bf16 v[66:69], a[28:31], v[110:117], v9 abid:1\n\t"
      "ds_read_b32 v125, v189 offset:2048\n\t"
      "v_smfmac_f32_16x16x64_bf16 v[54:57], a[32:35], v[70:77], v10\n\t"
      "ds_read_b128 v[102:105], v238 offset:2816\n\t"
      "v_cvt_pk_bf16_f32 v38, v38, v39\n\t"
      "v_smfmac_f32_16x16x64_bf16 v[54:57], a[48:51], v[78:85], v12\n\t"
      "ds_read_b32 v126, v190 offset:2048\n\t"
      "v_cvt_pk_bf16_f32 v39, v40, v41\n\t"
      "v_smfmac_f32_16x16x64_bf16 v[58:61], a[36:39], v[70:77], v10 abid:1\n\t"
      "s_waitcnt lgkmcnt(8)\n\t"
      "ds_read_b128 v[106:109], v239 offset:2816\n\t"
      "v_cvt_pk_bf16_f32 v40, v42, v43\n\t"
      "v_smfmac_f32_16x16x64_bf16 v[58:61], a[52:55], v[78:85], v12 abid:1\n\t"
      "ds_read_b32 v127, v191 offset:2048\n\t"
      "v_cvt_pk_bf16_f32 v41, v44, v45\n\t"
      "v_smfmac_f32_16x16x64_bf16 v[62:65], a[40:43], v[70:77], v11\n\t"
      "ds_read_b128 v[110:113], v240 offset:2816\n\t"
      "v_cvt_pk_bf16_f32 v46, v46, v47\n\t"
      "v_smfmac_f32_16x16x64_bf16 v[62:65], a[56:59], v[78:85], v13\n\t"
      "ds_read_b32 v128, v192 offset:2048\n\t"
      "v_cvt_pk_bf16_f32 v47, v48, v49\n\t"
      "v_smfmac_f32_16x16x64_bf16 v[66:69], a[44:47], v[70:77], v11 abid:1\n\t"
      "ds_read_b128 v[114:117], v241 offset:2816\n\t"
      "v_cvt_pk_bf16_f32 v48, v50, v51\n\t"
      "v_smfmac_f32_16x16x64_bf16 v[66:69], a[60:63], v[78:85], v13 abid:1\n\t"
      "ds_read_b32 v129, v193 offset:2048\n\t"
      "v_cvt_pk_bf16_f32 v49, v52, v53\n\t"
      "s_waitcnt lgkmcnt(13)\n\t"
      "v_smfmac_f32_16x16x64_bf16 v[54:57], a[64:67], v[86:93], v14\n\t"
      "ds_read_b128 v[70:73], v238 offset:3072\n\t"
      "v_mov_b32_dpp v42, v46 row_ror:8 row_mask:0xf bank_mask:0xf\n\t"
      "v_smfmac_f32_16x16x64_bf16 v[54:57], a[80:83], v[94:101], v16\n\t"
      "s_waitcnt lgkmcnt(8)\n\t"
      "ds_read_b32 v130, v194 offset:2048\n\t"
      "v_mov_b32_dpp v43, v47 row_ror:8 row_mask:0xf bank_mask:0xf\n\t"
      "v_smfmac_f32_16x16x64_bf16 v[58:61], a[68:71], v[86:93], v14 abid:1\n\t"
      "ds_read_b128 v[74:77], v239 offset:3072\n\t"
      "v_mov_b32_dpp v44, v48 row_ror:8 row_mask:0xf bank_mask:0xf\n\t"
      "v_smfmac_f32_16x16x64_bf16 v[58:61], a[84:87], v[94:101], v16 abid:1\n\t"
      "ds_read_b32 v131, v195 offset:2048\n\t"
      "v_mov_b32_dpp v45, v49 row_ror:8 row_mask:0xf bank_mask:0xf\n\t"
      "v_smfmac_f32_16x16x64_bf16 v[62:65], a[72:75], v[86:93], v15\n\t"
      "ds_read_b128 v[78:81], v240 offset:3072\n\t"
      "v_mov_b32_dpp v46, v38 row_ror:8 row_mask:0xf bank_mask:0x3\n\t"
      "v_smfmac_f32_16x16x64_bf16 v[62:65], a[88:91], v[94:101], v17\n\t"
      "ds_read_b32 v132, v196 offset:2048\n\t"
      "v_mov_b32_dpp v47, v39 row_ror:8 row_mask:0xf bank_mask:0x3\n\t"
      "v_smfmac_f32_16x16x64_bf16 v[66:69], a[76:79], v[86:93], v15 abid:1\n\t"
      "ds_read_b128 v[82:85], v241 offset:3072\n\t"
      "v_mov_b32_dpp v48, v40 row_ror:8 row_mask:0xf bank_mask:0x3\n\t"
      "v_smfmac_f32_16x16x64_bf16 v[66:69], a[92:95], v[94:101], v17 abid:1\n\t"
      "s_waitcnt lgkmcnt(8)\n\t"
      "ds_read_b32 v133, v197 offset:2048\n\t"
      "v_mov_b32_dpp v49, v41 row_ror:8 row_mask:0xf bank_mask:0x3\n\t"
      "s_waitcnt vmcnt(12)\n\t"
      "s_barrier\n\t"
      "s_mov_b32 s71, s67\n\t"
      "v_smfmac_f32_16x16x64_bf16 v[54:57], a[96:99], v[102:109], v18\n\t"
      "ds_read_b128 v[86:89], v238 offset:3328\n\t"
      "v_mov_b32_dpp v38, v42 quad_perm:[0,1,2,3] row_mask:0xf bank_mask:0xc\n\t"
      "s_add_u32 m0, s46, s71\n\t"
      "s_nop 0\n\t"
      "global_load_lds_dwordx4 %11, s[36:37]\n\t"
      "v_smfmac_f32_16x16x64_bf16 v[54:57], a[112:115], v[110:117], v20\n\t"
      "ds_read_b128 v[90:93], v239 offset:3328\n\t"
      "v_mov_b32_dpp v39, v43 quad_perm:[0,1,2,3] row_mask:0xf bank_mask:0xc\n\t"
      "v_smfmac_f32_16x16x64_bf16 v[58:61], a[100:103], v[102:109], v18 abid:1\n\t"
      "ds_read_b128 v[94:97], v240 offset:3328\n\t"
      "v_mov_b32_dpp v40, v44 quad_perm:[0,1,2,3] row_mask:0xf bank_mask:0xc\n\t"
      "v_smfmac_f32_16x16x64_bf16 v[58:61], a[116:119], v[110:117], v20 abid:1\n\t"
      "ds_read_b128 v[98:101], v241 offset:3328\n\t"
      "v_mov_b32_dpp v41, v45 quad_perm:[0,1,2,3] row_mask:0xf bank_mask:0xc\n\t"
      "global_load_lds_dwordx4 %11, s[36:37] offset:1024\n\t"
      "v_smfmac_f32_16x16x64_bf16 v[62:65], a[104:107], v[102:109], v19\n\t"
      "v_xor_b32 v166, s57, v166\n\t"
      "v_smfmac_f32_16x16x64_bf16 v[62:65], a[120:123], v[110:117], v21\n\t"
      "v_xor_b32 v167, s57, v167\n\t"
      "v_smfmac_f32_16x16x64_bf16 v[66:69], a[108:111], v[102:109], v19 abid:1\n\t"
      "v_xor_b32 v168, s57, v168\n\t"
      "s_add_u32 m0, s47, s71\n\t"
      "s_nop 0\n\t"
      "global_load_lds_dwordx4 %12, s[36:37]\n\t"
      "v_smfmac_f32_16x16x64_bf16 v[66:69], a[124:127], v[110:117], v21 abid:1\n\t"
      "v_xor_b32 v169, s57, v169\n\t"
      "v_xor_b32 v170, s57, v170\n\t"
      "v_xor_b32 v171, s57, v171\n\t"
      "v_xor_b32 v172, s57, v172\n\t"
      "v_xor_b32 v173, s57, v173\n\t"
      "v_xor_b32 v174, s57, v174\n\t"
      "v_xor_b32 v175, s57, v175\n\t"
      "v_xor_b32 v176, s57, v176\n\t"
      "v_xor_b32 v177, s57, v177\n\t"
      "v_xor_b32 v178, s57, v178\n\t"
      "v_xor_b32 v179, s57, v179\n\t"
      "v_xor_b32 v180, s57, v180\n\t"
      "v_xor_b32 v181, s57, v181\n\t"
      "s_waitcnt lgkmcnt(5)\n\t"
      "v_mfma_f32_16x16x32_bf16 v[54:57], v[150:153], v[118:121], v[54:57]\n\t"
      "ds_read_b128 v[102:105], v238 offset:3584\n\t"
      "v_mfma_f32_16x16x32_bf16 v[58:61], v[154:157], v[122:125], v[58:61]\n\t"
      "ds_read_b32 v230, v206 offset:2048\n\t"
      "global_load_lds_dwordx4 %12, s[36:37] offset:1024\n\t"
      "v_mfma_f32_16x16x32_bf16 v[62:65], v[158:161], v[126:129], v[62:65]\n\t"
      "ds_read_b128 v[106:109], v239 offset:3584\n\t"
      "s_waitcnt lgkmcnt(7)\n\t"
      "v_mfma_f32_16x16x32_bf16 v[66:69], v[162:165], v[130:133], v[66:69]\n\t"
      "ds_read_b32 v231, v207 offset:2048\n\t"
      "v_smfmac_f32_16x16x64_bf16 v[54:57], a[128:131], v[70:77], v22\n\t"
      "ds_read_b128 v[110:113], v240 offset:3584\n\t"
      "s_add_u32 m0, s48, s71\n\t"
      "s_nop 0\n\t"
      "global_load_lds_dwordx4 %13, s[36:37]\n\t"
      "v_smfmac_f32_16x16x64_bf16 v[54:57], a[144:147], v[78:85], v24\n\t"
      "ds_read_b32 v232, v208 offset:2048\n\t"
      "v_smfmac_f32_16x16x64_bf16 v[58:61], a[132:135], v[70:77], v22 abid:1\n\t"
      "ds_read_b128 v[114:117], v241 offset:3584\n\t"
      "v_smfmac_f32_16x16x64_bf16 v[58:61], a[148:151], v[78:85], v24 abid:1\n\t"
      "ds_read_b32 v233, v209 offset:2048\n\t"
      "global_load_lds_dwordx4 %13, s[36:37] offset:1024\n\t"
      "v_smfmac_f32_16x16x64_bf16 v[62:65], a[136:139], v[70:77], v23\n\t"
      "ds_read_b32 v234, v210 offset:2048\n\t"
      "v_smfmac_f32_16x16x64_bf16 v[62:65], a[152:155], v[78:85], v25\n\t"
      "ds_read_b32 v235, v211 offset:2048\n\t"
      "v_smfmac_f32_16x16x64_bf16 v[66:69], a[140:143], v[70:77], v23 abid:1\n\t"
      "s_waitcnt lgkmcnt(8)\n\t"
      "ds_read_b32 v236, v212 offset:2048\n\t"
      "s_add_u32 m0, s49, s71\n\t"
      "s_nop 0\n\t"
      "global_load_lds_dwordx4 %14, s[36:37]\n\t"
      "v_smfmac_f32_16x16x64_bf16 v[66:69], a[156:159], v[78:85], v25 abid:1\n\t"
      "ds_read_b32 v237, v213 offset:2048\n\t"
      "v_smfmac_f32_16x16x64_bf16 v[54:57], a[160:163], v[86:93], v26\n\t"
      "ds_read_b32 v214, v166 offset:0\n\t"
      "v_xor_b32 v182, s57, v182\n\t"
      "v_smfmac_f32_16x16x64_bf16 v[54:57], a[176:179], v[94:101], v28\n\t"
      "ds_read_b32 v215, v167 offset:0\n\t"
      "v_xor_b32 v183, s57, v183\n\t"
      "global_load_lds_dwordx4 %14, s[36:37] offset:1024\n\t"
      "s_cmp_lt_u32 s66, 5\n\t"
      "s_cselect_b32 s55, s73, 0\n\t"
      "s_add_u32 s36, s36, s73\n\t"
      "s_addc_u32 s37, s37, 0\n\t"
      "s_mov_b32 s73, s55\n\t"
      "v_smfmac_f32_16x16x64_bf16 v[58:61], a[164:167], v[86:93], v26 abid:1\n\t"
      "ds_read_b32 v216, v168 offset:0\n\t"
      "v_xor_b32 v184, s57, v184\n\t"
      "v_smfmac_f32_16x16x64_bf16 v[58:61], a[180:183], v[94:101], v28 abid:1\n\t"
      "s_mov_b64 exec, s[68:69]\n\t"
      "ds_read_b32 v217, v169 offset:0\n\t"
      "s_mov_b64 exec, -1\n\t"
      "v_xor_b32 v185, s57, v185\n\t"
      "v_smfmac_f32_16x16x64_bf16 v[62:65], a[168:171], v[86:93], v27\n\t"
      "s_waitcnt lgkmcnt(8)\n\t"
      "ds_read_b32 v218, v170 offset:0\n\t"
      "v_xor_b32 v186, s57, v186\n\t"
      "v_smfmac_f32_16x16x64_bf16 v[62:65], a[184:187], v[94:101], v29\n\t"
      "ds_read_b32 v219, v171 offset:0\n\t"
      "v_xor_b32 v187, s57, v187\n\t"
      "v_smfmac_f32_16x16x64_bf16 v[66:69], a[172:175], v[86:93], v27 abid:1\n\t"
      "ds_read_b32 v220, v172 offset:0\n\t"
      "v_xor_b32 v188, s57, v188\n\t"
      "v_smfmac_f32_16x16x64_bf16 v[66:69], a[188:191], v[94:101], v29 abid:1\n\t"
      "s_mov_b64 exec, s[68:69]\n\t"
      "ds_read_b32 v221, v173 offset:0\n\t"
      "s_mov_b64 exec, -1\n\t"
      "v_xor_b32 v189, s57, v189\n\t"
      "ds_read_b128 v[86:89], v238 offset:3840\n\t"
      "ds_read_b128 v[90:93], v239 offset:3840\n\t"
      "s_waitcnt lgkmcnt(8)\n\t"
      "ds_read_b128 v[94:97], v240 offset:3840\n\t"
      "ds_read_b128 v[98:101], v241 offset:3840\n\t"
      "v_xor_b32 v238, s57, v238\n\t"
      "v_xor_b32 v239, s57, v239\n\t"
      "v_xor_b32 v240, s57, v240\n\t"
      "v_xor_b32 v241, s57, v241\n\t"
      "v_mfma_f32_16x16x16_bf16 v[54:57], v[198:199], v[230:231], v[54:57]\n\t"
      "ds_read_b128 v[70:73], v238 offset:0\n\t"
      "v_xor_b32 v190, s57, v190\n\t"
      "v_mfma_f32_16x16x16_bf16 v[58:61], v[200:201], v[232:233], v[58:61]\n\t"
      "ds_read_b32 v222, v174 offset:0\n\t"
      "v_xor_b32 v191, s57, v191\n\t"
      "v_mfma_f32_16x16x16_bf16 v[62:65], v[202:203], v[234:235], v[62:65]\n\t"
      "ds_read_b128 v[74:77], v239 offset:0\n\t"
      "v_xor_b32 v192, s57, v192\n\t"
      "v_mfma_f32_16x16x16_bf16 v[66:69], v[204:205], v[236:237], v[66:69]\n\t"
      "ds_read_b32 v223, v175 offset:0\n\t"
      "v_xor_b32 v193, s57, v193\n\t"
      "v_smfmac_f32_16x16x64_bf16 v[54:57], a[192:195], v[102:109], v30\n\t"
      "s_waitcnt lgkmcnt(8)\n\t"
      "ds_read_b128 v[78:81], v240 offset:0\n\t"
      "v_xor_b32 v194, s57, v194\n\t"
      "v_smfmac_f32_16x16x64_bf16 v[54:57], a[208:211], v[110:117], v32\n\t"
      "ds_read_b32 v224, v176 offset:0\n\t"
      "v_xor_b32 v195, s57, v195\n\t"
      "v_smfmac_f32_16x16x64_bf16 v[58:61], a[196:199], v[102:109], v30 abid:1\n\t"
      "ds_read_b128 v[82:85], v241 offset:0\n\t"
      "v_xor_b32 v196, s57, v196\n\t"
      "v_smfmac_f32_16x16x64_bf16 v[58:61], a[212:215], v[110:117], v32 abid:1\n\t"
      "s_mov_b64 exec, s[68:69]\n\t"
      "ds_read_b32 v225, v177 offset:0\n\t"
      "s_mov_b64 exec, -1\n\t"
      "v_xor_b32 v197, s57, v197\n\t"
      "v_smfmac_f32_16x16x64_bf16 v[62:65], a[200:203], v[102:109], v31\n\t"
      "ds_read_b32 v226, v178 offset:0\n\t"
      "v_smfmac_f32_16x16x64_bf16 v[62:65], a[216:219], v[110:117], v33\n\t"
      "ds_read_b32 v227, v179 offset:0\n\t"
      "v_smfmac_f32_16x16x64_bf16 v[66:69], a[204:207], v[102:109], v31 abid:1\n\t"
      "s_waitcnt lgkmcnt(8)\n\t"
      "ds_read_b32 v228, v180 offset:0\n\t"
      "v_smfmac_f32_16x16x64_bf16 v[66:69], a[220:223], v[110:117], v33 abid:1\n\t"
      "s_mov_b64 exec, s[68:69]\n\t"
      "ds_read_b32 v229, v181 offset:0\n\t"
      "s_mov_b64 exec, -1\n\t"
      "s_cbranch_vccnz .Lgc1_%=\n\t"
      ".Lgr1_%=:\n\t"
      "v_xor_b32 v206, s57, v206\n\t"
      "v_xor_b32 v207, s57, v207\n\t"
      "v_xor_b32 v208, s57, v208\n\t"
      "v_xor_b32 v209, s57, v209\n\t"
      "v_xor_b32 v210, s57, v210\n\t"
      "v_xor_b32 v211, s57, v211\n\t"
      "v_xor_b32 v212, s57, v212\n\t"
      "v_xor_b32 v213, s57, v213\n\t"
      "v_smfmac_f32_16x16x64_bf16 v[54:57], a[224:227], v[86:93], v34\n\t"
      "global_store_dwordx4 v244, v[38:41], s[38:39] sc1\n\t"
      "v_smfmac_f32_16x16x64_bf16 v[54:57], a[240:243], v[94:101], v36\n\t"
      "v_smfmac_f32_16x16x64_bf16 v[58:61], a[228:231], v[86:93], v34 abid:1\n\t"
      "global_store_dwordx4 v252, v[46:49], s[38:39] sc1\n\t"
      "v_smfmac_f32_16x16x64_bf16 v[58:61], a[244:247], v[94:101], v36 abid:1\n\t"
      "v_smfmac_f32_16x16x64_bf16 v[62:65], a[232:235], v[86:93], v35\n\t"
      "s_add_u32 s38, s38, 2097152\n\t"
      "s_addc_u32 s39, s39, 0\n\t"
      "v_smfmac_f32_16x16x64_bf16 v[62:65], a[248:251], v[94:101], v37\n\t"
      "v_smfmac_f32_16x16x64_bf16 v[66:69], a[236:239], v[86:93], v35 abid:1\n\t"
      "v_smfmac_f32_16x16x64_bf16 v[66:69], a[252:255], v[94:101], v37 abid:1\n\t"
      "s_xor_b32 s67, s67, 4096\n\t"
      "s_add_u32 s66, s66, 1\n\t"
      "s_cmp_lt_u32 s66, 8\n\t"
      "s_cbranch_scc1 .LloopB%=\n\t"
      "s_branch .Lfin%=\n\t"
      ".Lep%=:\n\t"
      "v_cmp_ne_u32 vcc, s34, v242\n\t"
      "s_cbranch_vccz .Lpolled2%=\n\t"
      "s_waitcnt lgkmcnt(0)\n\t"
      "s_mov_b64 s[60:61], %8\n\t"
      "s_add_u32 s60, s60, 4096\n\t"
      "s_addc_u32 s61, s61, 0\n\t"
      "v_lshlrev_b32 v249, 3, %0\n\t"
      "s_mov_b32 s56, 200000\n\t"
      "v_add_u32 v250, 4096, v249\n\t"
      ".Lpoll2%=:\n\t"
      "global_load_dword v70, v249, s[60:61] offset:0 sc1\n\t"
      "global_load_dword v71, v249, s[60:61] offset:512 sc1\n\t"
      "global_load_dword v72, v249, s[60:61] offset:1024 sc1\n\t"
      "global_load_dword v73, v249, s[60:61] offset:1536 sc1\n\t"
      "global_load_dword v74, v249, s[60:61] offset:2048 sc1\n\t"
      "global_load_dword v75, v249, s[60:61] offset:2560 sc1\n\t"
      "global_load_dword v76, v249, s[60:61] offset:3072 sc1\n\t"
      "global_load_dword v77, v249, s[60:61] offset:3584 sc1\n\t"
      "global_load_dword v78, v250, s[60:61] offset:0 sc1\n\t"
      "global_load_dword v79, v250, s[60:61] offset:512 sc1\n\t"
      "global_load_dword v80, v250, s[60:61] offset:1024 sc1\n\t"
      "global_load_dword v81, v250, s[60:61] offset:1536 sc1\n\t"
      "global_load_dword v82, v250, s[60:61] offset:2048 sc1\n\t"
      "global_load_dword v83, v250, s[60:61] offset:2560 sc1\n\t"
      "global_load_dword v84, v250, s[60:61] offset:3072 sc1\n\t"
      "global_load_dword v85, v250, s[60:61] offset:3584 sc1\n\t"
      "s_waitcnt vmcnt(0)\n\t"
      "v_cmp_ne_u32 s[62:63], s34, v70\n\t"
      "v_cmp_ne_u32 vcc, s34, v71\n\t"
      "s_or_b64 s[62:63], s[62:63], vcc\n\t"
      "v_cmp_ne_u32 vcc, s34, v72\n\t"
      "s_or_b64 s[62:63], s[62:63], vcc\n\t"
      "v_cmp_ne_u32 vcc, s34, v73\n\t"
      "s_or_b64 s[62:63], s[62:63], vcc\n\t"
      "v_cmp_ne_u32 vcc, s34, v74\n\t"
      "s_or_b64 s[62:63], s[62:63], vcc\n\t"
      "v_cmp_ne_u32 vcc, s34, v75\n\t"
      "s_or_b64 s[62:63], s[62:63], vcc\n\t"
      "v_cmp_ne_u32 vcc, s34, v76\n\t"
      "s_or_b64 s[62:63], s[62:63], vcc\n\t"
      "v_cmp_ne_u32 vcc, s34, v77\n\t"
      "s_or_b64 s[62:63], s[62:63], vcc\n\t"
      "v_cmp_ne_u32 vcc, s34, v78\n\t"
      "s_or_b64 s[62:63], s[62:63], vcc\n\t"
      "v_cmp_ne_u32 vcc, s34, v79\n\t"
      "s_or_b64 s[62:63], s[62:63], vcc\n\t"
      "v_cmp_ne_u32 vcc, s34, v80\n\t"
      "s_or_b64 s[62:63], s[62:63], vcc\n\t"
      "v_cmp_ne_u32 vcc, s34, v81\n\t"
      "s_or_b64 s[62:63], s[62:63], vcc\n\t"
      "v_cmp_ne_u32 vcc, s34, v82\n\t"
      "s_or_b64 s[62:63], s[62:63], vcc\n\t"
      "v_cmp_ne_u32 vcc, s34, v83\n\t"
      "s_or_b64 s[62:63], s[62:63], vcc\n\t"
      "v_cmp_ne_u32 vcc, s34, v84\n\t"
      "s_or_b64 s[62:63], s[62:63], vcc\n\t"
      "v_cmp_ne_u32 vcc, s34, v85\n\t"
      "s_or_b64 s[62:63], s[62:63], vcc\n\t"
      "s_cmp_eq_u64 s[62:63], 0\n\t"
      "s_cbranch_scc1 .Lpolled2%=\n\t"
      "s_sleep 2\n\t"
      "s_sub_u32 s56, s56, 1\n\t"
      "s_cmp_lg_u32 s56, 0\n\t"
      "s_cbranch_scc1 .Lpoll2%=\n\t"
      ".Lpolled2%=:\n\t"
      "v_mov_b32 v230, s34\n\t"
      "v_mov_b32 v231, s35\n\t"
      "v_mov_b32 v249, 0\n\t"
      "s_mov_b64 exec, 1\n\t"
      "global_store_dwordx2 v249, v[230:231], %8 sc1\n\t"
      "s_mov_b64 exec, -1\n\t"
      "s_branch .Lnoep%=\n\t"
      ".Lseen%=:\n\t"
      "s_lshr_b32 s55, %10, 1\n\t"
      "v_mov_b32 v249, s55\n\t"
      "v_mov_b32 v250, s34\n\t"
      "s_mov_b64 exec, 1\n\t"
      "global_store_dword v249, v250, %8 offset:2048 sc1\n\t"
      "s_mov_b64 exec, -1\n\t"
      "s_branch .Lseenr%=\n\t"
      ".Lco0_%=:\n\t"
      "v_cmp_gt_u32 vcc, 40, v250\n\t"
      "s_mov_b64 s[60:61], exec\n\t"
      "s_andn2_b64 exec, s[60:61], vcc\n\t"
      "s_cbranch_execz .Lcn0_%=\n\t"
      "v_subrev_u32 v242, 40, v250\n\t"
      "v_lshrrev_b32 v231, 4, v242\n\t"
      "v_lshlrev_b32 v231, 5, v231\n\t"
      "v_bfe_u32 v243, v242, 2, 2\n\t"
      "v_lshl_add_u32 v231, v243, 1, v231\n\t"
      "v_and_b32 v242, 3, v242\n\t"
      "v_lshl_add_u32 v231, v242, 3, v231\n\t"
      "s_waitcnt vmcnt(20) lgkmcnt(8)\n\t"
      "global_store_short v231, v251, s[44:45] offset:0\n\t"
      ".Lcn0_%=:\n\t"
      "s_and_b64 exec, s[60:61], vcc\n\t"
      "s_cbranch_execz .Lcs0_%=\n\t"
      "s_branch .Lcr0_%=\n\t"
      ".Lco1_%=:\n\t"
      "v_cmp_gt_u32 vcc, 40, v250\n\t"
      "s_mov_b64 s[60:61], exec\n\t"
      "s_andn2_b64 exec, s[60:61], vcc\n\t"
      "s_cbranch_execz .Lcn1_%=\n\t"
      "v_subrev_u32 v242, 40, v250\n\t"
      "v_lshrrev_b32 v231, 4, v242\n\t"
      "v_lshlrev_b32 v231, 5, v231\n\t"
      "v_bfe_u32 v243, v242, 2, 2\n\t"
      "v_lshl_add_u32 v231, v243, 1, v231\n\t"
      "v_and_b32 v242, 3, v242\n\t"
      "v_lshl_add_u32 v231, v242, 3, v231\n\t"
      "s_waitcnt vmcnt(20) lgkmcnt(8)\n\t"
      "global_store_short v231, v251, s[44:45] offset:1024\n\t"
      ".Lcn1_%=:\n\t"
      "s_and_b64 exec, s[60:61], vcc\n\t"
      "s_cbranch_execz .Lcs1_%=\n\t"
      "s_branch .Lcr1_%=\n\t"
      ".Lco2_%=:\n\t"
      "v_cmp_gt_u32 vcc, 40, v250\n\t"
      "s_mov_b64 s[60:61], exec\n\t"
      "s_andn2_b64 exec, s[60:61], vcc\n\t"
      "s_cbranch_execz .Lcn2_%=\n\t"
      "v_subrev_u32 v242, 40, v250\n\t"
      "v_lshrrev_b32 v231, 4, v242\n\t"
      "v_lshlrev_b32 v231, 5, v231\n\t"
      "v_bfe_u32 v243, v242, 2, 2\n\t"
      "v_lshl_add_u32 v231, v243, 1, v231\n\t"
      "v_and_b32 v242, 3, v242\n\t"
      "v_lshl_add_u32 v231, v242, 3, v231\n\t"
      "s_waitcnt vmcnt(20) lgkmcnt(8)\n\t"
      "global_store_short v231, v251, s[44:45] offset:2048\n\t"
      ".Lcn2_%=:\n\t"
      "s_and_b64 exec, s[60:61], vcc\n\t"
      "s_cbranch_execz .Lcs2_%=\n\t"
      "s_branch .Lcr2_%=\n\t"
      ".Lco3_%=:\n\t"
      "v_cmp_gt_u32 vcc, 40, v250\n\t"
      "s_mov_b64 s[60:61], exec\n\t"
      "s_andn2_b64 exec, s[60:61], vcc\n\t"
      "s_cbranch_execz .Lcn3_%=\n\t"
      "v_subrev_u32 v242, 40, v250\n\t"
      "v_lshrrev_b32 v231, 4, v242\n\t"
      "v_lshlrev_b32 v231, 5, v231\n\t"
      "v_bfe_u32 v243, v242, 2, 2\n\t"
      "v_lshl_add_u32 v231, v243, 1, v231\n\t"
      "v_and_b32 v242, 3, v242\n\t"
      "v_lshl_add_u32 v231, v242, 3, v231\n\t"
      "s_waitcnt vmcnt(20) lgkmcnt(8)\n\t"
      "global_store_short v231, v251, s[44:45] offset:3072\n\t"
      ".Lcn3_%=:\n\t"
      "s_and_b64 exec, s[60:61], vcc\n\t"
      "s_cbranch_execz .Lcs3_%=\n\t"
      "s_branch .Lcr3_%=\n\t"
      ".Lgc0_%=:\n\t"
      "s_add_u32 s59, s67, 0\n\t"
      "s_call_b64 s[64:65], .Lrare0_%=\n\t"
      "s_branch .Lgr0_%=\n\t"
      ".Lgc1_%=:\n\t"
      "s_add_u32 s59, s67, 2048\n\t"
      "s_call_b64 s[64:65], .Lrare1_%=\n\t"
      "s_branch .Lgr1_%=\n\t"
      ".Lgp0_%=:\n\t"
      "s_movk_i32 s59, 0\n\t"
      "s_call_b64 s[64:65], .Lrare0_%=\n\t"
      "s_branch .Lgq0_%=\n\t"
      ".Lgp1_%=:\n\t"
      "s_movk_i32 s59, 2048\n\t"
      "s_call_b64 s[64:65], .Lrare1_%=\n\t"
      "s_branch .Lgq1_%=\n\t"
      ".Lrare0_%=:\n\t"
      "s_waitcnt vmcnt(0) lgkmcnt(0)\n\t"
      "s_nop 7\n\t"
      "v_mov_b32 v251, 512\n\t"
      "s_mov_b32 s55, 40\n\t"
      "s_add_u32 s60, s44, 0\n\t"
      "s_addc_u32 s61, s45, 0\n\t"
      "s_add_u32 s62, s42, 0\n\t"
      "s_addc_u32 s63, s43, 0\n\t"
      ".Lrl0_0_%=:\n\t"
      "s_cmp_le_u32 s50, s55\n\t"
      "s_cbranch_scc1 .Lrd0_0_%=\n\t"
      "v_lshlrev_b32 v249, 3, v248\n\t"
      "global_load_dwordx2 v[242:243], v249, s[60:61] sc0 sc1\n\t"
      "s_waitcnt vmcnt(0)\n\t"
      "v_and_b32 v249, 0xffff, v242\n\t"
      "v_add_u32 v250, 0, v248\n\t"
      "v_add_u32 v250, s55, v250\n\t"
      "v_cmp_gt_u32 vcc, s50, v250\n\t"
      "v_lshlrev_b32 v250, 2, v249\n\t"
      "v_xor_b32 v250, v250, v246\n\t"
      "v_cndmask_b32 v249, v251, v249, vcc\n\t"
      "v_cndmask_b32 v250, 0, v250, vcc\n\t"
      "v_add_u32 v250, s59, v250\n\t"
      "ds_read_b32 v234, v250\n\t"
      "v_lshl_add_u32 v249, v249, 6, v247\n\t"
      "global_load_dword v230, v249, s[62:63] sc1\n\t"
      "v_lshrrev_b32 v249, 16, v242\n\t"
      "v_add_u32 v250, 4, v248\n\t"
      "v_add_u32 v250, s55, v250\n\t"
      "v_cmp_gt_u32 vcc, s50, v250\n\t"
      "v_lshlrev_b32 v250, 2, v249\n\t"
      "v_xor_b32 v250, v250, v246\n\t"
      "v_cndmask_b32 v249, v251, v249, vcc\n\t"
      "v_cndmask_b32 v250, 0, v250, vcc\n\t"
      "v_add_u32 v250, s59, v250\n\t"
      "ds_read_b32 v235, v250\n\t"
      "v_lshl_add_u32 v249, v249, 6, v247\n\t"
      "global_load_dword v231, v249, s[62:63] sc1\n\t"
      "v_and_b32 v249, 0xffff, v243\n\t"
      "v_add_u32 v250, 8, v248\n\t"
      "v_add_u32 v250, s55, v250\n\t"
      "v_cmp_gt_u32 vcc, s50, v250\n\t"
      "v_lshlrev_b32 v250, 2, v249\n\t"
      "v_xor_b32 v250, v250, v246\n\t"
      "v_cndmask_b32 v249, v251, v249, vcc\n\t"
      "v_cndmask_b32 v250, 0, v250, vcc\n\t"
      "v_add_u32 v250, s59, v250\n\t"
      "ds_read_b32 v236, v250\n\t"
      "v_lshl_add_u32 v249, v249, 6, v247\n\t"
      "global_load_dword v232, v249, s[62:63] sc1\n\t"
      "v_lshrrev_b32 v249, 16, v243\n\t"
      "v_add_u32 v250, 12, v248\n\t"
      "v_add_u32 v250, s55, v250\n\t"
      "v_cmp_gt_u32 vcc, s50, v250\n\t"
      "v_lshlrev_b32 v250, 2, v249\n\t"
      "v_xor_b32 v250, v250, v246\n\t"
      "v_cndmask_b32 v249, v251, v249, vcc\n\t"
      "v_cndmask_b32 v250, 0, v250, vcc\n\t"
      "v_add_u32 v250, s59, v250\n\t"
      "ds_read_b32 v237, v250\n\t"
      "v_lshl_add_u32 v249, v249, 6, v247\n\t"
      "global_load_dword v233, v249, s[62:63] sc1\n\t"
      "s_waitcnt vmcnt(0) lgkmcnt(0)\n\t"
      "v_mfma_f32_16x16x32_bf16 v[38:41], v[230:233], v[234:237], v[38:41]\n\t"
      "s_add_u32 s55, s55, 16\n\t"
      "s_add_u32 s60, s60, 32\n\t"
      "s_addc_u32 s61, s61, 0\n\t"
      "s_branch .Lrl0_0_%=\n\t"
      ".Lrd0_0_%=:\n\t"
      "s_mov_b32 s55, 40\n\t"
      "s_add_u32 s60, s44, 1024\n\t"
      "s_addc_u32 s61, s45, 0\n\t"
      "s_add_u32 s62, s42, 32896\n\t"
      "s_addc_u32 s63, s43, 0\n\t"
      ".Lrl0_1_%=:\n\t"
      "s_cmp_le_u32 s51, s55\n\t"
      "s_cbranch_scc1 .Lrd0_1_%=\n\t"
      "v_lshlrev_b32 v249, 3, v248\n\t"
      "global_load_dwordx2 v[242:243], v249, s[60:61] sc0 sc1\n\t"
      "s_waitcnt vmcnt(0)\n\t"
      "v_and_b32 v249, 0xffff, v242\n\t"
      "v_add_u32 v250, 0, v248\n\t"
      "v_add_u32 v250, s55, v250\n\t"
      "v_cmp_gt_u32 vcc, s51, v250\n\t"
      "v_lshlrev_b32 v250, 2, v249\n\t"
      "v_xor_b32 v250, v250, v246\n\t"
      "v_cndmask_b32 v249, v251, v249, vcc\n\t"
      "v_cndmask_b32 v250, 0, v250, vcc\n\t"
      "v_add_u32 v250, s59, v250\n\t"
      "ds_read_b32 v234, v250\n\t"
      "v_lshl_add_u32 v249, v249, 6, v247\n\t"
      "global_load_dword v230, v249, s[62:63] sc1\n\t"
      "v_lshrrev_b32 v249, 16, v242\n\t"
      "v_add_u32 v250, 4, v248\n\t"
      "v_add_u32 v250, s55, v250\n\t"
      "v_cmp_gt_u32 vcc, s51, v250\n\t"
      "v_lshlrev_b32 v250, 2, v249\n\t"
      "v_xor_b32 v250, v250, v246\n\t"
      "v_cndmask_b32 v249, v251, v249, vcc\n\t"
      "v_cndmask_b32 v250, 0, v250, vcc\n\t"
      "v_add_u32 v250, s59, v250\n\t"
      "ds_read_b32 v235, v250\n\t"
      "v_lshl_add_u32 v249, v249, 6, v247\n\t"
      "global_load_dword v231, v249, s[62:63] sc1\n\t"
      "v_and_b32 v249, 0xffff, v243\n\t"
      "v_add_u32 v250, 8, v248\n\t"
      "v_add_u32 v250, s55, v250\n\t"
      "v_cmp_gt_u32 vcc, s51, v250\n\t"
      "v_lshlrev_b32 v250, 2, v249\n\t"
      "v_xor_b32 v250, v250, v246\n\t"
      "v_cndmask_b32 v249, v251, v249, vcc\n\t"
      "v_cndmask_b32 v250, 0, v250, vcc\n\t"
      "v_add_u32 v250, s59, v250\n\t"
      "ds_read_b32 v236, v250\n\t"
      "v_lshl_add_u32 v249, v249, 6, v247\n\t"
      "global_load_dword v232, v249, s[62:63] sc1\n\t"
      "v_lshrrev_b32 v249, 16, v243\n\t"
      "v_add_u32 v250, 12, v248\n\t"
      "v_add_u32 v250, s55, v250\n\t"
      "v_cmp_gt_u32 vcc, s51, v250\n\t"
      "v_lshlrev_b32 v250, 2, v249\n\t"
      "v_xor_b32 v250, v250, v246\n\t"
      "v_cndmask_b32 v249, v251, v249, vcc\n\t"
      "v_cndmask_b32 v250, 0, v250, vcc\n\t"
      "v_add_u32 v250, s59, v250\n\t"
      "ds_read_b32 v237, v250\n\t"
      "v_lshl_add_u32 v249, v249, 6, v247\n\t"
      "global_load_dword v233, v249, s[62:63] sc1\n\t"
      "s_waitcnt vmcnt(0) lgkmcnt(0)\n\t"
      "v_mfma_f32_16x16x32_bf16 v[42:45], v[230:233], v[234:237], v[42:45]\n\t"
      "s_add_u32 s55, s55, 16\n\t"
      "s_add_u32 s60, s60, 32\n\t"
      "s_addc_u32 s61, s61, 0\n\t"
      "s_branch .Lrl0_1_%=\n\t"
      ".Lrd0_1_%=:\n\t"
      "s_mov_b32 s55, 40\n\t"
      "s_add_u32 s60, s44, 2048\n\t"
      "s_addc_u32 s61, s45, 0\n\t"
      "s_add_u32 s62, s42, 65792\n\t"
      "s_addc_u32 s63, s43, 0\n\t"
      ".Lrl0_2_%=:\n\t"
      "s_cmp_le_u32 s52, s55\n\t"
      "s_cbranch_scc1 .Lrd0_2_%=\n\t"
      "v_lshlrev_b32 v249, 3, v248\n\t"
      "global_load_dwordx2 v[242:243], v249, s[60:61] sc0 sc1\n\t"
      "s_waitcnt vmcnt(0)\n\t"
      "v_and_b32 v249, 0xffff, v242\n\t"
      "v_add_u32 v250, 0, v248\n\t"
      "v_add_u32 v250, s55, v250\n\t"
      "v_cmp_gt_u32 vcc, s52, v250\n\t"
      "v_lshlrev_b32 v250, 2, v249\n\t"
      "v_xor_b32 v250, v250, v246\n\t"
      "v_cndmask_b32 v249, v251, v249, vcc\n\t"
      "v_cndmask_b32 v250, 0, v250, vcc\n\t"
      "v_add_u32 v250, s59, v250\n\t"
      "ds_read_b32 v234, v250\n\t"
      "v_lshl_add_u32 v249, v249, 6, v247\n\t"
      "global_load_dword v230, v249, s[62:63] sc1\n\t"
      "v_lshrrev_b32 v249, 16, v242\n\t"
      "v_add_u32 v250, 4, v248\n\t"
      "v_add_u32 v250, s55, v250\n\t"
      "v_cmp_gt_u32 vcc, s52, v250\n\t"
      "v_lshlrev_b32 v250, 2, v249\n\t"
      "v_xor_b32 v250, v250, v246\n\t"
      "v_cndmask_b32 v249, v251, v249, vcc\n\t"
      "v_cndmask_b32 v250, 0, v250, vcc\n\t"
      "v_add_u32 v250, s59, v250\n\t"
      "ds_read_b32 v235, v250\n\t"
      "v_lshl_add_u32 v249, v249, 6, v247\n\t"
      "global_load_dword v231, v249, s[62:63] sc1\n\t"
      "v_and_b32 v249, 0xffff, v243\n\t"
      "v_add_u32 v250, 8, v248\n\t"
      "v_add_u32 v250, s55, v250\n\t"
      "v_cmp_gt_u32 vcc, s52, v250\n\t"
      "v_lshlrev_b32 v250, 2, v249\n\t"
      "v_xor_b32 v250, v250, v246\n\t"
      "v_cndmask_b32 v249, v251, v249, vcc\n\t"
      "v_cndmask_b32 v250, 0, v250, vcc\n\t"
      "v_add_u32 v250, s59, v250\n\t"
      "ds_read_b32 v236, v250\n\t"
      "v_lshl_add_u32 v249, v249, 6, v247\n\t"
      "global_load_dword v232, v249, s[62:63] sc1\n\t"
      "v_lshrrev_b32 v249, 16, v243\n\t"
      "v_add_u32 v250, 12, v248\n\t"
      "v_add_u32 v250, s55, v250\n\t"
      "v_cmp_gt_u32 vcc, s52, v250\n\t"
      "v_lshlrev_b32 v250, 2, v249\n\t"
      "v_xor_b32 v250, v250, v246\n\t"
      "v_cndmask_b32 v249, v251, v249, vcc\n\t"
      "v_cndmask_b32 v250, 0, v250, vcc\n\t"
      "v_add_u32 v250, s59, v250\n\t"
      "ds_read_b32 v237, v250\n\t"
      "v_lshl_add_u32 v249, v249, 6, v247\n\t"
      "global_load_dword v233, v249, s[62:63] sc1\n\t"
      "s_waitcnt vmcnt(0) lgkmcnt(0)\n\t"
      "v_mfma_f32_16x16x32_bf16 v[46:49], v[230:233], v[234:237], v[46:49]\n\t"
      "s_add_u32 s55, s55, 16\n\t"
      "s_add_u32 s60, s60, 32\n\t"
      "s_addc_u32 s61, s61, 0\n\t"
      "s_branch .Lrl0_2_%=\n\t"
      ".Lrd0_2_%=:\n\t"
      "s_mov_b32 s55, 40\n\t"
      "s_add_u32 s60, s44, 3072\n\t"
      "s_addc_u32 s61, s45, 0\n\t"
      "s_add_u32 s62, s42, 98688\n\t"
      "s_addc_u32 s63, s43, 0\n\t"
      ".Lrl0_3_%=:\n\t"
      "s_cmp_le_u32 s53, s55\n\t"
      "s_cbranch_scc1 .Lrd0_3_%=\n\t"
      "v_lshlrev_b32 v249, 3, v248\n\t"
      "global_load_dwordx2 v[242:243], v249, s[60:61] sc0 sc1\n\t"
      "s_waitcnt vmcnt(0)\n\t"
      "v_and_b32 v249, 0xffff, v242\n\t"
      "v_add_u32 v250, 0, v248\n\t"
      "v_add_u32 v250, s55, v250\n\t"
      "v_cmp_gt_u32 vcc, s53, v250\n\t"
      "v_lshlrev_b32 v250, 2, v249\n\t"
      "v_xor_b32 v250, v250, v246\n\t"
      "v_cndmask_b32 v249, v251, v249, vcc\n\t"
      "v_cndmask_b32 v250, 0, v250, vcc\n\t"
      "v_add_u32 v250, s59, v250\n\t"
      "ds_read_b32 v234, v250\n\t"
      "v_lshl_add_u32 v249, v249, 6, v247\n\t"
      "global_load_dword v230, v249, s[62:63] sc1\n\t"
      "v_lshrrev_b32 v249, 16, v242\n\t"
      "v_add_u32 v250, 4, v248\n\t"
      "v_add_u32 v250, s55, v250\n\t"
      "v_cmp_gt_u32 vcc, s53, v250\n\t"
      "v_lshlrev_b32 v250, 2, v249\n\t"
      "v_xor_b32 v250, v250, v246\n\t"
      "v_cndmask_b32 v249, v251, v249, vcc\n\t"
      "v_cndmask_b32 v250, 0, v250, vcc\n\t"
      "v_add_u32 v250, s59, v250\n\t"
      "ds_read_b32 v235, v250\n\t"
      "v_lshl_add_u32 v249, v249, 6, v247\n\t"
      "global_load_dword v231, v249, s[62:63] sc1\n\t"
      "v_and_b32 v249, 0xffff, v243\n\t"
      "v_add_u32 v250, 8, v248\n\t"
      "v_add_u32 v250, s55, v250\n\t"
      "v_cmp_gt_u32 vcc, s53, v250\n\t"
      "v_lshlrev_b32 v250, 2, v249\n\t"
      "v_xor_b32 v250, v250, v246\n\t"
      "v_cndmask_b32 v249, v251, v249, vcc\n\t"
      "v_cndmask_b32 v250, 0, v250, vcc\n\t"
      "v_add_u32 v250, s59, v250\n\t"
      "ds_read_b32 v236, v250\n\t"
      "v_lshl_add_u32 v249, v249, 6, v247\n\t"
      "global_load_dword v232, v249, s[62:63] sc1\n\t"
      "v_lshrrev_b32 v249, 16, v243\n\t"
      "v_add_u32 v250, 12, v248\n\t"
      "v_add_u32 v250, s55, v250\n\t"
      "v_cmp_gt_u32 vcc, s53, v250\n\t"
      "v_lshlrev_b32 v250, 2, v249\n\t"
      "v_xor_b32 v250, v250, v246\n\t"
      "v_cndmask_b32 v249, v251, v249, vcc\n\t"
      "v_cndmask_b32 v250, 0, v250, vcc\n\t"
      "v_add_u32 v250, s59, v250\n\t"
      "ds_read_b32 v237, v250\n\t"
      "v_lshl_add_u32 v249, v249, 6, v247\n\t"
      "global_load_dword v233, v249, s[62:63] sc1\n\t"
      "s_waitcnt vmcnt(0) lgkmcnt(0)\n\t"
      "v_mfma_f32_16x16x32_bf16 v[50:53], v[230:233], v[234:237], v[50:53]\n\t"
      "s_add_u32 s55, s55, 16\n\t"
      "s_add_u32 s60, s60, 32\n\t"
      "s_addc_u32 s61, s61, 0\n\t"
      "s_branch .Lrl0_3_%=\n\t"
      ".Lrd0_3_%=:\n\t"
      "s_mov_b64 vcc, -1\n\t"
      "s_nop 7\n\t"
      "s_setpc_b64 s[64:65]\n\t"
      ".Lrare1_%=:\n\t"
      "s_waitcnt vmcnt(0) lgkmcnt(0)\n\t"
      "s_nop 7\n\t"
      "v_mov_b32 v251, 512\n\t"
      "s_mov_b32 s55, 40\n\t"
      "s_add_u32 s60, s44, 0\n\t"
      "s_addc_u32 s61, s45, 0\n\t"
      "s_add_u32 s62, s42, 0\n\t"
      "s_addc_u32 s63, s43, 0\n\t"
      ".Lrl1_0_%=:\n\t"
      "s_cmp_le_u32 s50, s55\n\t"
      "s_cbranch_scc1 .Lrd1_0_%=\n\t"
      "v_lshlrev_b32 v249, 3, v248\n\t"
      "global_load_dwordx2 v[242:243], v249, s[60:61] sc0 sc1\n\t"
      "s_waitcnt vmcnt(0)\n\t"
      "v_and_b32 v249, 0xffff, v242\n\t"
      "v_add_u32 v250, 0, v248\n\t"
      "v_add_u32 v250, s55, v250\n\t"
      "v_cmp_gt_u32 vcc, s50, v250\n\t"
      "v_lshlrev_b32 v250, 2, v249\n\t"
      "v_xor_b32 v250, v250, v246\n\t"
      "v_cndmask_b32 v249, v251, v249, vcc\n\t"
      "v_cndmask_b32 v250, 0, v250, vcc\n\t"
      "v_add_u32 v250, s59, v250\n\t"
      "ds_read_b32 v234, v250\n\t"
      "v_lshl_add_u32 v249, v249, 6, v247\n\t"
      "global_load_dword v230, v249, s[62:63] sc1\n\t"
      "v_lshrrev_b32 v249, 16, v242\n\t"
      "v_add_u32 v250, 4, v248\n\t"
      "v_add_u32 v250, s55, v250\n\t"
      "v_cmp_gt_u32 vcc, s50, v250\n\t"
      "v_lshlrev_b32 v250, 2, v249\n\t"
      "v_xor_b32 v250, v250, v246\n\t"
      "v_cndmask_b32 v249, v251, v249, vcc\n\t"
      "v_cndmask_b32 v250, 0, v250, vcc\n\t"
      "v_add_u32 v250, s59, v250\n\t"
      "ds_read_b32 v235, v250\n\t"
      "v_lshl_add_u32 v249, v249, 6, v247\n\t"
      "global_load_dword v231, v249, s[62:63] sc1\n\t"
      "v_and_b32 v249, 0xffff, v243\n\t"
      "v_add_u32 v250, 8, v248\n\t"
      "v_add_u32 v250, s55, v250\n\t"
      "v_cmp_gt_u32 vcc, s50, v250\n\t"
      "v_lshlrev_b32 v250, 2, v249\n\t"
      "v_xor_b32 v250, v250, v246\n\t"
      "v_cndmask_b32 v249, v251, v249, vcc\n\t"
      "v_cndmask_b32 v250, 0, v250, vcc\n\t"
      "v_add_u32 v250, s59, v250\n\t"
      "ds_read_b32 v236, v250\n\t"
      "v_lshl_add_u32 v249, v249, 6, v247\n\t"
      "global_load_dword v232, v249, s[62:63] sc1\n\t"
      "v_lshrrev_b32 v249, 16, v243\n\t"
      "v_add_u32 v250, 12, v248\n\t"
      "v_add_u32 v250, s55, v250\n\t"
      "v_cmp_gt_u32 vcc, s50, v250\n\t"
      "v_lshlrev_b32 v250, 2, v249\n\t"
      "v_xor_b32 v250, v250, v246\n\t"
      "v_cndmask_b32 v249, v251, v249, vcc\n\t"
      "v_cndmask_b32 v250, 0, v250, vcc\n\t"
      "v_add_u32 v250, s59, v250\n\t"
      "ds_read_b32 v237, v250\n\t"
      "v_lshl_add_u32 v249, v249, 6, v247\n\t"
      "global_load_dword v233, v249, s[62:63] sc1\n\t"
      "s_waitcnt vmcnt(0) lgkmcnt(0)\n\t"
      "v_mfma_f32_16x16x32_bf16 v[54:57], v[230:233], v[234:237], v[54:57]\n\t"
      "s_add_u32 s55, s55, 16\n\t"
      "s_add_u32 s60, s60, 32\n\t"
      "s_addc_u32 s61, s61, 0\n\t"
      "s_branch .Lrl1_0_%=\n\t"
      ".Lrd1_0_%=:\n\t"
      "s_mov_b32 s55, 40\n\t"
      "s_add_u32 s60, s44, 1024\n\t"
      "s_addc_u32 s61, s45, 0\n\t"
      "s_add_u32 s62, s42, 32896\n\t"
      "s_addc_u32 s63, s43, 0\n\t"
      ".Lrl1_1_%=:\n\t"
      "s_cmp_le_u32 s51, s55\n\t"
      "s_cbranch_scc1 .Lrd1_1_%=\n\t"
      "v_lshlrev_b32 v249, 3, v248\n\t"
      "global_load_dwordx2 v[242:243], v249, s[60:61] sc0 sc1\n\t"
      "s_waitcnt vmcnt(0)\n\t"
      "v_and_b32 v249, 0xffff, v242\n\t"
      "v_add_u32 v250, 0, v248\n\t"
      "v_add_u32 v250, s55, v250\n\t"
      "v_cmp_gt_u32 vcc, s51, v250\n\t"
      "v_lshlrev_b32 v250, 2, v249\n\t"
      "v_xor_b32 v250, v250, v246\n\t"
      "v_cndmask_b32 v249, v251, v249, vcc\n\t"
      "v_cndmask_b32 v250, 0, v250, vcc\n\t"
      "v_add_u32 v250, s59, v250\n\t"
      "ds_read_b32 v234, v250\n\t"
      "v_lshl_add_u32 v249, v249, 6, v247\n\t"
      "global_load_dword v230, v249, s[62:63] sc1\n\t"
      "v_lshrrev_b32 v249, 16, v242\n\t"
      "v_add_u32 v250, 4, v248\n\t"
      "v_add_u32 v250, s55, v250\n\t"
      "v_cmp_gt_u32 vcc, s51, v250\n\t"
      "v_lshlrev_b32 v250, 2, v249\n\t"
      "v_xor_b32 v250, v250, v246\n\t"
      "v_cndmask_b32 v249, v251, v249, vcc\n\t"
      "v_cndmask_b32 v250, 0, v250, vcc\n\t"
      "v_add_u32 v250, s59, v250\n\t"
      "ds_read_b32 v235, v250\n\t"
      "v_lshl_add_u32 v249, v249, 6, v247\n\t"
      "global_load_dword v231, v249, s[62:63] sc1\n\t"
      "v_and_b32 v249, 0xffff, v243\n\t"
      "v_add_u32 v250, 8, v248\n\t"
      "v_add_u32 v250, s55, v250\n\t"
      "v_cmp_gt_u32 vcc, s51, v250\n\t"
      "v_lshlrev_b32 v250, 2, v249\n\t"
      "v_xor_b32 v250, v250, v246\n\t"
      "v_cndmask_b32 v249, v251, v249, vcc\n\t"
      "v_cndmask_b32 v250, 0, v250, vcc\n\t"
      "v_add_u32 v250, s59, v250\n\t"
      "ds_read_b32 v236, v250\n\t"
      "v_lshl_add_u32 v249, v249, 6, v247\n\t"
      "global_load_dword v232, v249, s[62:63] sc1\n\t"
      "v_lshrrev_b32 v249, 16, v243\n\t"
      "v_add_u32 v250, 12, v248\n\t"
      "v_add_u32 v250, s55, v250\n\t"
      "v_cmp_gt_u32 vcc, s51, v250\n\t"
      "v_lshlrev_b32 v250, 2, v249\n\t"
      "v_xor_b32 v250, v250, v246\n\t"
      "v_cndmask_b32 v249, v251, v249, vcc\n\t"
      "v_cndmask_b32 v250, 0, v250, vcc\n\t"
      "v_add_u32 v250, s59, v250\n\t"
      "ds_read_b32 v237, v250\n\t"
      "v_lshl_add_u32 v249, v249, 6, v247\n\t"
      "global_load_dword v233, v249, s[62:63] sc1\n\t"
      "s_waitcnt vmcnt(0) lgkmcnt(0)\n\t"
      "v_mfma_f32_16x16x32_bf16 v[58:61], v[230:233], v[234:237], v[58:61]\n\t"
      "s_add_u32 s55, s55, 16\n\t"
      "s_add_u32 s60, s60, 32\n\t"
      "s_addc_u32 s61, s61, 0\n\t"
      "s_branch .Lrl1_1_%=\n\t"
      ".Lrd1_1_%=:\n\t"
      "s_mov_b32 s55, 40\n\t"
      "s_add_u32 s60, s44, 2048\n\t"
      "s_addc_u32 s61, s45, 0\n\t"
      "s_add_u32 s62, s42, 65792\n\t"
      "s_addc_u32 s63, s43, 0\n\t"
      ".Lrl1_2_%=:\n\t"
      "s_cmp_le_u32 s52, s55\n\t"
      "s_cbranch_scc1 .Lrd1_2_%=\n\t"
      "v_lshlrev_b32 v249, 3, v248\n\t"
      "global_load_dwordx2 v[242:243], v249, s[60:61] sc0 sc1\n\t"
      "s_waitcnt vmcnt(0)\n\t"
      "v_and_b32 v249, 0xffff, v242\n\t"
      "v_add_u32 v250, 0, v248\n\t"
      "v_add_u32 v250, s55, v250\n\t"
      "v_cmp_gt_u32 vcc, s52, v250\n\t"
      "v_lshlrev_b32 v250, 2, v249\n\t"
      "v_xor_b32 v250, v250, v246\n\t"
      "v_cndmask_b32 v249, v251, v249, vcc\n\t"
      "v_cndmask_b32 v250, 0, v250, vcc\n\t"
      "v_add_u32 v250, s59, v250\n\t"
      "ds_read_b32 v234, v250\n\t"
      "v_lshl_add_u32 v249, v249, 6, v247\n\t"
      "global_load_dword v230, v249, s[62:63] sc1\n\t"
      "v_lshrrev_b32 v249, 16, v242\n\t"
      "v_add_u32 v250, 4, v248\n\t"
      "v_add_u32 v250, s55, v250\n\t"
      "v_cmp_gt_u32 vcc, s52, v250\n\t"
      "v_lshlrev_b32 v250, 2, v249\n\t"
      "v_xor_b32 v250, v250, v246\n\t"
      "v_cndmask_b32 v249, v251, v249, vcc\n\t"
      "v_cndmask_b32 v250, 0, v250, vcc\n\t"
      "v_add_u32 v250, s59, v250\n\t"
      "ds_read_b32 v235, v250\n\t"
      "v_lshl_add_u32 v249, v249, 6, v247\n\t"
      "global_load_dword v231, v249, s[62:63] sc1\n\t"
      "v_and_b32 v249, 0xffff, v243\n\t"
      "v_add_u32 v250, 8, v248\n\t"
      "v_add_u32 v250, s55, v250\n\t"
      "v_cmp_gt_u32 vcc, s52, v250\n\t"
      "v_lshlrev_b32 v250, 2, v249\n\t"
      "v_xor_b32 v250, v250, v246\n\t"
      "v_cndmask_b32 v249, v251, v249, vcc\n\t"
      "v_cndmask_b32 v250, 0, v250, vcc\n\t"
      "v_add_u32 v250, s59, v250\n\t"
      "ds_read_b32 v236, v250\n\t"
      "v_lshl_add_u32 v249, v249, 6, v247\n\t"
      "global_load_dword v232, v249, s[62:63] sc1\n\t"
      "v_lshrrev_b32 v249, 16, v243\n\t"
      "v_add_u32 v250, 12, v248\n\t"
      "v_add_u32 v250, s55, v250\n\t"
      "v_cmp_gt_u32 vcc, s52, v250\n\t"
      "v_lshlrev_b32 v250, 2, v249\n\t"
      "v_xor_b32 v250, v250, v246\n\t"
      "v_cndmask_b32 v249, v251, v249, vcc\n\t"
      "v_cndmask_b32 v250, 0, v250, vcc\n\t"
      "v_add_u32 v250, s59, v250\n\t"
      "ds_read_b32 v237, v250\n\t"
      "v_lshl_add_u32 v249, v249, 6, v247\n\t"
      "global_load_dword v233, v249, s[62:63] sc1\n\t"
      "s_waitcnt vmcnt(0) lgkmcnt(0)\n\t"
      "v_mfma_f32_16x16x32_bf16 v[62:65], v[230:233], v[234:237], v[62:65]\n\t"
      "s_add_u32 s55, s55, 16\n\t"
      "s_add_u32 s60, s60, 32\n\t"
      "s_addc_u32 s61, s61, 0\n\t"
      "s_branch .Lrl1_2_%=\n\t"
      ".Lrd1_2_%=:\n\t"
      "s_mov_b32 s55, 40\n\t"
      "s_add_u32 s60, s44, 3072\n\t"
      "s_addc_u32 s61, s45, 0\n\t"
      "s_add_u32 s62, s42, 98688\n\t"
      "s_addc_u32 s63, s43, 0\n\t"
      ".Lrl1_3_%=:\n\t"
      "s_cmp_le_u32 s53, s55\n\t"
      "s_cbranch_scc1 .Lrd1_3_%=\n\t"
      "v_lshlrev_b32 v249, 3, v248\n\t"
      "global_load_dwordx2 v[242:243], v249, s[60:61] sc0 sc1\n\t"
      "s_waitcnt vmcnt(0)\n\t"
      "v_and_b32 v249, 0xffff, v242\n\t"
      "v_add_u32 v250, 0, v248\n\t"
      "v_add_u32 v250, s55, v250\n\t"
      "v_cmp_gt_u32 vcc, s53, v250\n\t"
      "v_lshlrev_b32 v250, 2, v249\n\t"
      "v_xor_b32 v250, v250, v246\n\t"
      "v_cndmask_b32 v249, v251, v249, vcc\n\t"
      "v_cndmask_b32 v250, 0, v250, vcc\n\t"
      "v_add_u32 v250, s59, v250\n\t"
      "ds_read_b32 v234, v250\n\t"
      "v_lshl_add_u32 v249, v249, 6, v247\n\t"
      "global_load_dword v230, v249, s[62:63] sc1\n\t"
      "v_lshrrev_b32 v249, 16, v242\n\t"
      "v_add_u32 v250, 4, v248\n\t"
      "v_add_u32 v250, s55, v250\n\t"
      "v_cmp_gt_u32 vcc, s53, v250\n\t"
      "v_lshlrev_b32 v250, 2, v249\n\t"
      "v_xor_b32 v250, v250, v246\n\t"
      "v_cndmask_b32 v249, v251, v249, vcc\n\t"
      "v_cndmask_b32 v250, 0, v250, vcc\n\t"
      "v_add_u32 v250, s59, v250\n\t"
      "ds_read_b32 v235, v250\n\t"
      "v_lshl_add_u32 v249, v249, 6, v247\n\t"
      "global_load_dword v231, v249, s[62:63] sc1\n\t"
      "v_and_b32 v249, 0xffff, v243\n\t"
      "v_add_u32 v250, 8, v248\n\t"
      "v_add_u32 v250, s55, v250\n\t"
      "v_cmp_gt_u32 vcc, s53, v250\n\t"
      "v_lshlrev_b32 v250, 2, v249\n\t"
      "v_xor_b32 v250, v250, v246\n\t"
      "v_cndmask_b32 v249, v251, v249, vcc\n\t"
      "v_cndmask_b32 v250, 0, v250, vcc\n\t"
      "v_add_u32 v250, s59, v250\n\t"
      "ds_read_b32 v236, v250\n\t"
      "v_lshl_add_u32 v249, v249, 6, v247\n\t"
      "global_load_dword v232, v249, s[62:63] sc1\n\t"
      "v_lshrrev_b32 v249, 16, v243\n\t"
      "v_add_u32 v250, 12, v248\n\t"
      "v_add_u32 v250, s55, v250\n\t"
      "v_cmp_gt_u32 vcc, s53, v250\n\t"
      "v_lshlrev_b32 v250, 2, v249\n\t"
      "v_xor_b32 v250, v250, v246\n\t"
      "v_cndmask_b32 v249, v251, v249, vcc\n\t"
      "v_cndmask_b32 v250, 0, v250, vcc\n\t"
      "v_add_u32 v250, s59, v250\n\t"
      "ds_read_b32 v237, v250\n\t"
      "v_lshl_add_u32 v249, v249, 6, v247\n\t"
      "global_load_dword v233, v249, s[62:63] sc1\n\t"
      "s_waitcnt vmcnt(0) lgkmcnt(0)\n\t"
      "v_mfma_f32_16x16x32_bf16 v[66:69], v[230:233], v[234:237], v[66:69]\n\t"
      "s_add_u32 s55, s55, 16\n\t"
      "s_add_u32 s60, s60, 32\n\t"
      "s_addc_u32 s61, s61, 0\n\t"
      "s_branch .Lrl1_3_%=\n\t"
      ".Lrd1_3_%=:\n\t"
      "s_mov_b64 vcc, -1\n\t"
      "s_nop 7\n\t"
      "s_setpc_b64 s[64:65]\n\t"
      ".Lend%=:\n\t"
      :
      : "v"(lane), "s"(wv), "s"(xb), "s"(ob), "s"(blob), "s"(rv), "s"(gl), "s"(slab), "s"(ws), "s"(fw), "s"(bid),
        "v"(voff[0]), "v"(voff[1]), "v"(voff[2]), "v"(voff[3]), "s"(tag)
      : "v6", "v7", "v8", "v9", "v10", "v11", "v12", "v13", "v14", "v15", "v16", "v17", "v18", "v19", "v20", "v21", "v22", "v23", "v24", "v25", "v26", "v27", "v28", "v29", "v30", "v31", "v32", "v33", "v34", "v35", "v36", "v37", "v38", "v39", "v40", "v41", "v42", "v43", "v44", "v45", "v46", "v47", "v48", "v49", "v50", "v51", "v52", "v53", "v54", "v55", "v56", "v57", "v58", "v59", "v60", "v61", "v62", "v63", "v64", "v65", "v66", "v67", "v68", "v69", "v70", "v71", "v72", "v73", "v74", "v75", "v76", "v77", "v78", "v79", "v80", "v81", "v82", "v83", "v84", "v85", "v86", "v87", "v88", "v89", "v90", "v91", "v92", "v93", "v94", "v95", "v96", "v97", "v98", "v99", "v100", "v101", "v102", "v103", "v104", "v105", "v106", "v107", "v108", "v109", "v110", "v111", "v112", "v113", "v114", "v115", "v116", "v117", "v118", "v119", "v120", "v121", "v122", "v123", "v124", "v125", "v126", "v127", "v128", "v129", "v130", "v131", "v132", "v133", "v134", "v135", "v136", "v137", "v138", "v139", "v140", "v141", "v142", "v143", "v144", "v145", "v146", "v147", "v148", "v149", "v150", "v151", "v152", "v153", "v154", "v155", "v156", "v157", "v158", "v159", "v160", "v161", "v162", "v163", "v164", "v165", "v166", "v167", "v168", "v169", "v170", "v171", "v172", "v173", "v174", "v175", "v176", "v177", "v178", "v179", "v180", "v181", "v182", "v183", "v184", "v185", "v186", "v187", "v188", "v189", "v190", "v191", "v192", "v193", "v194", "v195", "v196", "v197", "v198", "v199", "v200", "v201", "v202", "v203", "v204", "v205", "v206", "v207", "v208", "v209", "v210", "v211", "v212", "v213", "v214", "v215", "v216", "v217", "v218", "v219", "v220", "v221", "v222", "v223", "v224", "v225", "v226", "v227", "v228", "v229", "v230", "v231", "v232", "v233", "v234", "v235", "v236", "v237", "v238", "v239", "v240", "v241", "v242", "v243", "v244", "v245", "v246", "v247", "v248", "v249", "v250", "v251", "v252", "v253", "v254", "v255", "a0", "a1", "a2", "a3", "a4", "a5", "a6", "a7", "a8", "a9", "a10", "a11", "a12", "a13", "a14", "a15", "a16", "a17", "a18", "a19", "a20", "a21", "a22", "a23", "a24", "a25", "a26", "a27", "a28", "a29", "a30", "a31", "a32", "a33", "a34", "a35", "a36", "a37", "a38", "a39", "a40", "a41", "a42", "a43", "a44", "a45", "a46", "a47", "a48", "a49", "a50", "a51", "a52", "a53", "a54", "a55", "a56", "a57", "a58", "a59", "a60", "a61", "a62", "a63", "a64", "a65", "a66", "a67", "a68", "a69", "a70", "a71", "a72", "a73", "a74", "a75", "a76", "a77", "a78", "a79", "a80", "a81", "a82", "a83", "a84", "a85", "a86", "a87", "a88", "a89", "a90", "a91", "a92", "a93", "a94", "a95", "a96", "a97", "a98", "a99", "a100", "a101", "a102", "a103", "a104", "a105", "a106", "a107", "a108", "a109", "a110", "a111", "a112", "a113", "a114", "a115", "a116", "a117", "a118", "a119", "a120", "a121", "a122", "a123", "a124", "a125", "a126", "a127", "a128", "a129", "a130", "a131", "a132", "a133", "a134", "a135", "a136", "a137", "a138", "a139", "a140", "a141", "a142", "a143", "a144", "a145", "a146", "a147", "a148", "a149", "a150", "a151", "a152", "a153", "a154", "a155", "a156", "a157", "a158", "a159", "a160", "a161", "a162", "a163", "a164", "a165", "a166", "a167", "a168", "a169", "a170", "a171", "a172", "a173", "a174", "a175", "a176", "a177", "a178", "a179", "a180", "a181", "a182", "a183", "a184", "a185", "a186", "a187", "a188", "a189", "a190", "a191", "a192", "a193", "a194", "a195", "a196", "a197", "a198", "a199", "a200", "a201", "a202", "a203", "a204", "a205", "a206", "a207", "a208", "a209", "a210", "a211", "a212", "a213", "a214", "a215", "a216", "a217", "a218", "a219", "a220", "a221", "a222", "a223", "a224", "a225", "a226", "a227", "a228", "a229", "a230", "a231", "a232", "a233", "a234", "a235", "a236", "a237", "a238", "a239", "a240", "a241", "a242", "a243", "a244", "a245", "a246", "a247", "a248", "a249", "a250", "a251", "a252", "a253", "a254", "a255", "s34", "s35", "s36", "s37", "s38", "s39", "s40", "s41", "s42", "s43", "s44", "s45", "s46", "s47", "s48", "s49", "s50", "s51", "s52", "s53", "s54", "s55", "s56", "s57", "s58", "s59", "s60", "s61", "s62", "s63", "s64", "s65", "s66", "s67", "s68", "s69", "s70", "s71", "s72", "s73", "s74", "s75", "s76", "s77", "s78", "s79", "s80", "s81", "s82", "s83", "s84", "s85", "s86", "s87", "s88", "s89", "s90", "s91", "s92", "s93", "s94", "s95", "s96", "s97", "s98", "s99", "vcc", "scc", "memory");
}

extern "C" void kernel_launch(void* const* d_in, const int* in_sizes, int n_in,
                              void* d_out, int out_size, void* d_ws, size_t ws_size,
                              hipStream_t stream) {
  (void)hipFuncSetAttribute((const void*)fused_kernel, hipFuncAttributeMaxDynamicSharedMemorySize, LDS_TOTAL);
  fused_kernel<<<256, 256, LDS_TOTAL, stream>>>((const uint16_t*)d_in[0], (const uint16_t*)d_in[1], (const uint16_t*)d_in[2],
                                                (unsigned char*)d_ws, (uint16_t*)d_out);
}
```
